```python
import torch

def _softmax(x, axis=-1):
    return torch.softmax(x, dim=axis)
def _gelu(x):
    return torch.nn.functional.gelu(x, approximate="tanh")
def _silu(x):
    return torch.nn.functional.silu(x)
def _sigmoid(x):
    return torch.sigmoid(x)
def _layernorm(x, axis=-1, eps=1e-5):
    m = x.mean(dim=axis, keepdim=True)
    v = x.var(dim=axis, keepdim=True, unbiased=False)
    return (x - m) / torch.sqrt(v + eps)
def _scatter_add(tgt, idx, src, axis=0):
    return tgt.clone().index_add_(axis, idx, src)
def _segment_sum(src, seg, n):
    out = torch.zeros((n,) + src.shape[1:], dtype=src.dtype)
    return out.index_add_(0, seg, src)
def _pad(x, pw):
    flat = tuple(v for pair in reversed(pw) for v in pair)
    return torch.nn.functional.pad(x, flat)

kernel_name = 'standalone__sliding_block_cached__decode'

def setup_inputs():

    _w = (lambda a: (lambda t: t.to(torch.bfloat16) if t.is_floating_point() else t)(torch.as_tensor(a)))
    B, Sq, D = 2, 1, 512
    Hq, Hkv, Sk, Dh = 32, 8, 32768, 128
    assert Hq % Hkv == 0, (Hq, Hkv)
    return {
        "x": _w((lambda *s: torch.randn(*s, dtype=torch.bfloat16) * 0.8)(B, Sq, D)),
        "k_cache": _w((lambda *s: torch.randn(*s, dtype=torch.bfloat16) * 0.8)(B, Hkv, Sk, Dh)),
        "v_cache": _w((lambda *s: torch.randn(*s, dtype=torch.bfloat16) * 0.05)(B, Hkv, Sk, Dh)),
        "wq": _w((lambda *s: torch.randn(*s, dtype=torch.bfloat16) * 0.8)(D, Hq * Dh) * (D ** -0.5)),
        "wo": _w((lambda *s: torch.randn(*s, dtype=torch.bfloat16) * 0.05)(Hq * Dh, D) * ((Hq * Dh) ** -0.5)),
    }

def reference(x, k_cache, v_cache, wq, wo):
    B, Sq, D = x.shape
    Hq, Dh = 32, 128
    Hkv, Sk = k_cache.shape[1], k_cache.shape[2]
    W = min(4096, max(256, Sk // 4))
    rep = Hq // Hkv
    q = (lambda x, *d: x.permute(*d))((x @ wq).reshape(B, Sq, Hq, Dh), 0, 2, 1, 3)
    k = (lambda x, n, axis: torch.repeat_interleave(x, n, dim=axis))(k_cache[:, :, Sk - W:, :], rep, axis=1)
    v = (lambda x, n, axis: torch.repeat_interleave(x, n, dim=axis))(v_cache[:, :, Sk - W:, :], rep, axis=1)
    scores = torch.einsum("bhqd,bhkd->bhqk", q, k) * (Dh ** -0.5)
    p = _softmax(scores, axis=-1)
    o = torch.einsum("bhqk,bhkd->bhqd", p, v)
    return (lambda x, *d: x.permute(*d))(o, 0, 2, 1, 3).reshape(B, Sq, Hq * Dh) @ wo
```

```cpp
#include <hip/hip_runtime.h>
#include <hip/hip_bf16.h>

typedef unsigned v4u __attribute__((ext_vector_type(4)));
typedef unsigned v2u __attribute__((ext_vector_type(2)));
typedef float v4f __attribute__((ext_vector_type(4)));
typedef float v2f __attribute__((ext_vector_type(2)));
typedef short v4s __attribute__((ext_vector_type(4)));
typedef __bf16 v8bf __attribute__((ext_vector_type(8)));
typedef __bf16 v2bf __attribute__((ext_vector_type(2)));
typedef unsigned long long v8q __attribute__((ext_vector_type(8)));
#define LAS __attribute__((address_space(3)))
#define GAS __attribute__((address_space(1)))
#define CAS __attribute__((address_space(4)))
#define SB() __builtin_amdgcn_sched_barrier(0)
#define MEMBAR() asm volatile("" ::: "memory")
#define WAITVM(n) do { SB(); __builtin_amdgcn_s_waitcnt((((n) & 15) | (((n) >> 4) << 14) | (7 << 4) | (15 << 8))); SB(); } while (0)

#define DEPTH 8
#define D0 4
#define GPOS 18
#define QPOS 24
#define WOAT 5
#define NOB7 0
#define PUBW 4
constexpr int SK = 32768, WIN = 4096, DH = 128;
constexpr int AUX_NT = 2, AUX_SC1 = 16;
constexpr unsigned RSRC_FLAGS = 0x00020000u;
constexpr unsigned WS_EPOCH = 0;
constexpr unsigned WS_QP = 4096;
constexpr unsigned WS_OP = WS_QP + 8 * 2 * 4 * 2048;
constexpr unsigned WS_FP = WS_OP + 8 * 2 * 16 * 4096;
constexpr unsigned WS_END = WS_FP + 4 * 64 * 128 * 16;
constexpr unsigned WS_STAMP = 4u << 20;
constexpr int L_W = 0;
constexpr int L_MRG = 16384;
constexpr int L_Q = L_MRG + 4 * 4 * 128 * 4;
constexpr int L_MAX = L_Q + 1024;
constexpr int L_SUM = L_MAX + 64;
constexpr int L_O = L_SUM + 64;
constexpr int L_X = L_O + 256;
constexpr int L_END = L_X + 512;
constexpr unsigned SPIN_CAP = 1u << 18;

__device__ __forceinline__ v4f mfma16(v4u a, v4u b, v4f c) {
  return __builtin_amdgcn_mfma_f32_16x16x32_bf16(__builtin_bit_cast(v8bf, a), __builtin_bit_cast(v8bf, b), c, 0, 0, 0);
}
__device__ __forceinline__ unsigned pkbf(float lo, float hi) {
  v2bf r; r[0] = (__bf16)lo; r[1] = (__bf16)hi; return __builtin_bit_cast(unsigned, r);
}
template <int CTRL> __device__ __forceinline__ float dpp(float x) {
  return __builtin_bit_cast(float, __builtin_amdgcn_mov_dpp(__builtin_bit_cast(int, x), CTRL, 0xf, 0xf, true));
}
constexpr int SHL4 = 0x104, SHL8 = 0x108, SHL12 = 0x10c, SHR4 = 0x114, SHR8 = 0x118, SHR12 = 0x11c, ROR8 = 0x128;
#define F(x) __uint_as_float(x)
#define U(x) __float_as_uint(x)
__device__ __forceinline__ float vmax(float a, float b) { return __builtin_elementwise_maximum(a, b); }
__device__ __forceinline__ float fold32(float a, float b) { auto t = __builtin_amdgcn_permlane32_swap(U(a), U(b), false, false); return F(t[0]) + F(t[1]); }
__device__ __forceinline__ float fold16(float a, float b) { auto t = __builtin_amdgcn_permlane16_swap(U(a), U(b), false, false); return F(t[0]) + F(t[1]); }
__device__ __forceinline__ float foldmax32(float a, float b) { auto t = __builtin_amdgcn_permlane32_swap(U(a), U(b), false, false); return vmax(F(t[0]), F(t[1])); }
__device__ __forceinline__ float foldmax16(float a, float b) { auto t = __builtin_amdgcn_permlane16_swap(U(a), U(b), false, false); return vmax(F(t[0]), F(t[1])); }
__device__ __forceinline__ v4u tr_frag(LAS unsigned char* p, int second_off) {
  const v4s a = __builtin_amdgcn_ds_read_tr16_b64_v4i16((LAS v4s*)p);
  const v4s b = __builtin_amdgcn_ds_read_tr16_b64_v4i16((LAS v4s*)(p + second_off));
  const v2u a2 = __builtin_bit_cast(v2u, a), b2 = __builtin_bit_cast(v2u, b);
  return v4u{a2[0], a2[1], b2[0], b2[1]};
}
__device__ __forceinline__ void lds_barrier() {
  SB();
  asm volatile("s_waitcnt lgkmcnt(0)" ::: "memory");
  __builtin_amdgcn_s_barrier();
  MEMBAR();
  SB();
}
__device__ __forceinline__ unsigned badtag(v4u d, unsigned tag) { return (d[1] ^ tag) | (d[3] ^ tag); }
template <unsigned RND>
__device__ __forceinline__ v4u pack4(v4f a, unsigned tag) {
  const unsigned r0 = U(a[0]) + RND, r1 = U(a[1]) + RND, r2 = U(a[2]) + RND, r3 = U(a[3]) + RND;
  return v4u{__builtin_amdgcn_perm(r0, r1, 0x07060503u), __builtin_amdgcn_perm(r1, tag, 0x06050100u),
             __builtin_amdgcn_perm(r2, r3, 0x07060503u), __builtin_amdgcn_perm(r3, tag, 0x06050100u)};
}
__device__ __forceinline__ v4f unpack4(v4u d) {
  return v4f{F(d[0] & 0xffffff00u), F(__builtin_amdgcn_perm(d[0], d[1], 0x0403020cu)), F(d[2] & 0xffffff00u), F(__builtin_amdgcn_perm(d[2], d[3], 0x0403020cu))};
}
__device__ __forceinline__ unsigned sel(unsigned long long m, unsigned t, unsigned f) { return __builtin_amdgcn_inverse_ballot_w64(m) ? t : f; }
#define STAMP(i) do {} while (0)
#define COUNT(x) do {} while (0)
#define DUMP() do {} while (0)

__global__ void __launch_bounds__(512, 2)
decode_fused(const void* x_, const void* kc_, const void* vc_, const void* wq_, const void* wo_, void* out_, void* ws_, unsigned long long pad_) {
  __shared__ __attribute__((aligned(16))) unsigned char lds_raw[L_END];
  LAS unsigned char* lds = (LAS unsigned char*)lds_raw;
  STAMP(0);
  v8q ka;
  asm volatile("s_load_dwordx16 %0, %1, 0x0" : "=s"(ka) : "s"(__builtin_amdgcn_kernarg_segment_ptr()));
  SB();
  const int bid = blockIdx.x, g = bid & 7, j = bid >> 3, b = j >> 4, s = j & 15;
  const int w = __builtin_amdgcn_readfirstlane(threadIdx.x >> 6), l = threadIdx.x & 63;
  const int g4 = l >> 4, bank = (l >> 2) & 3;
  const int gn = (g + 1) & 7;
  const int rq = j >> 3, cq = j & 7;
  const int r8 = j >> 2, hh = r8 >> 1, half = r8 & 1, c = j & 3;
  unsigned wq_v = (l >> 3) * 8192 + 16 * (l & 7), wq_s = (128 * rq + 32 * w) * 8192 + gn * 1024 + 128 * cq;
  unsigned x_v = (l & 4) * 256 + 16 * (l & 3), x_s = 256 * rq + 64 * w;
  unsigned koff = 256 * bank + 64 * g4 + 16 * (l & 3);
  unsigned long long kvoff = ((unsigned long long)(b * 8 + g) * SK + (SK - WIN) + s * 256) * DH * 2;
  asm volatile("" : "+v"(wq_v), "+v"(x_v), "+v"(koff));
  asm volatile("" : "+s"(wq_s), "+s"(x_s), "+s"(kvoff));
  SB();
  asm volatile("s_waitcnt lgkmcnt(0)" : "+s"(ka));
  const __amdgpu_buffer_rsrc_t rws = __builtin_amdgcn_make_buffer_rsrc((void*)ka[6], 0, WS_END, RSRC_FLAGS);
  unsigned tag;
  unsigned v_h1, s_h1, a_ow, a_or, a_tb, s_fp;
  auto tail_addresses = [&]() __attribute__((always_inline)) {
    v_h1 = (l >> 3) * 4096 + 16 * (l & 7);
    s_h1 = WS_OP + ((gn * 2 + (w >> 2)) * 16 * 4 + hh) * 1024 + (4 * half + (w & 3)) * 128;
    a_ow = L_O + (w >> 2) * 128 + (16 * (w & 3) + 4 * (l & 3) + 2 * (l >> 5) + (g4 & 1)) * 2;
    a_or = L_O + (l & 1) * 128 + 16 * g4;
    a_tb = L_W + (8 * g4 + bank) * 256 + 8 * (l & 3) + 32 * w;
    s_fp = WS_FP + ((c * 64 + gn * 8 + r8) * 128 + 16 * w) * 16;
    asm volatile("" : "+v"(v_h1), "+v"(a_ow), "+v"(a_or), "+v"(a_tb));
    asm volatile("" : "+s"(s_h1), "+s"(s_fp));
  };

  const unsigned wo_s = (gn * 512 + 64 * r8 + 16 * (w & 3)) * 1024 + 256 * c;
  auto issue_wo = [&]() __attribute__((always_inline)) {
    const __amdgpu_buffer_rsrc_t ro = __builtin_amdgcn_make_buffer_rsrc((void*)ka[4], 0, 4096 * 512 * 2, RSRC_FLAGS);
#pragma unroll
    for (int i = 0; i < 4; ++i)
      __builtin_amdgcn_raw_ptr_buffer_load_lds(ro, lds + L_W + (w & 3) * 4096 + i * 1024, 16, g4 * 1024 + 16 * (l & 15), wo_s + i * 4096, 0, AUX_NT);
    SB();
  };
  if (__builtin_expect(w < 4, 1)) {
    const __amdgpu_buffer_rsrc_t rq_ = __builtin_amdgcn_make_buffer_rsrc((void*)ka[3], 0, 512 * 4096 * 2, RSRC_FLAGS);
    const __amdgpu_buffer_rsrc_t rx = __builtin_amdgcn_make_buffer_rsrc((void*)ka[0], 0, 2 * 512 * 2, RSRC_FLAGS);
    v4u wv[4], kk[16], vv[16];
#pragma unroll
    for (int i = 0; i < 4; ++i) { wv[i] = __builtin_amdgcn_raw_buffer_load_b128(rq_, wq_v, wq_s + i * 65536, AUX_NT); SB(); }
    const v4u xv = __builtin_amdgcn_raw_buffer_load_b128(rx, x_v, x_s, 0);
    SB();
    const __amdgpu_buffer_rsrc_t rk = __builtin_amdgcn_make_buffer_rsrc((void*)(ka[1] + kvoff), 0, 65536, RSRC_FLAGS);
    const __amdgpu_buffer_rsrc_t rv = __builtin_amdgcn_make_buffer_rsrc((void*)(ka[2] + kvoff), 0, 65536, RSRC_FLAGS);
    auto stream = [&](int m) __attribute__((always_inline)) {
      if (m < 16) kk[m] = __builtin_amdgcn_raw_buffer_load_b128(rk, koff, (((m + s) & 15) * 4 + w) * 1024, AUX_NT);
      else vv[m - 16] = __builtin_amdgcn_raw_buffer_load_b128(rv, 16 * l, (((m + s) & 15) * 4 + w) * 1024, AUX_NT);
      SB();
    };
    __builtin_amdgcn_s_barrier();
    SB();
#pragma unroll
    for (int m = 0; m < D0; ++m) stream(m);
    tag = *(const CAS unsigned*)(ka[6] + WS_EPOCH) + 1u;
    STAMP(1);

#pragma unroll
    for (int i = 0; i < 4; ++i) { *(LAS v4u*)(lds + L_W + (4 * w + i) * 1024 + 16 * l) = wv[i]; SB(); stream(D0 + i); }
    *(LAS v4u*)(lds + L_X + (l & 4) * 64 + 64 * w + 16 * (l & 3)) = xv;
    SB();
    stream(D0 + 4);
    STAMP(2);
    lds_barrier();
    {
      LAS unsigned char* tb = lds + L_W + (8 * g4 + bank) * 128 + 8 * (l & 3) + 32 * w;
      v4u f[4], xa[4];
#pragma unroll
      for (int ks = 0; ks < 4; ++ks) { f[ks] = tr_frag(tb + ks * 32 * 128, 4 * 128); xa[ks] = *(LAS v4u*)(lds + L_X + (l & 1) * 256 + 64 * ks + 16 * g4); }
      SB();
      v4f qa = mfma16(f[0], xa[0], v4f{0.f, 0.f, 0.f, 0.f}), qb = mfma16(f[1], xa[1], v4f{0.f, 0.f, 0.f, 0.f});
      qa = mfma16(f[2], xa[2], qa); qb = mfma16(f[3], xa[3], qb);
      if ((l & 15) < 2) *(LAS v4u*)(lds + L_MRG + (l & 1) * 256 + 64 * w + 16 * g4) = pack4<0x80u>(qa + qb, tag);
      lds_barrier();
      if (PUBW == 0 && w == 0 && l < 32)
        __builtin_amdgcn_raw_buffer_store_b128(*(LAS v4u*)(lds + L_MRG + 16 * l), rws, (l >> 4) * 8192 + 16 * (l & 15), WS_QP + (gn * 8 + rq) * 2048 + 256 * cq, AUX_SC1);
      SB();
    }
    STAMP(3);
    tail_addresses();
    unsigned a_qf = L_Q + (l & 3) * 256 + 16 * (4 * g4 + bank);
    unsigned a_mw = L_MRG + (w * 4 * 128 + 8 * (l & 15) + 2 * g4) * 4, a_mr = L_MRG + (w * 128 + 16 * (l >> 3) + 4 * (l & 3)) * 4, a_ls = L_SUM + 4 * w;
    unsigned s_op = WS_OP + (((g * 2 + b) * 16 + s) * 4 + w) * 1024;
    unsigned long long m_b0 = __ballot(bank == 0), m_ml = __ballot(l & 4);
    asm volatile("" : "+v"(a_qf), "+v"(a_mw), "+v"(a_mr), "+v"(a_ls));
    asm volatile("" : "+s"(s_op), "+s"(m_b0), "+s"(m_ml));

    v4u qg[2];
    auto gather = [&]() __attribute__((always_inline)) {
      MEMBAR();
#pragma unroll
      for (int i = 0; i < 2; ++i)
        qg[i] = __builtin_amdgcn_raw_buffer_load_b128(rws, (l >> 5) * 2048 + 16 * (l & 31), WS_QP + (g * 2 + b) * 8192 + i * 4096 + w * 512, AUX_SC1);
      SB();
      COUNT(rounds0);
    };
    auto gather_bad = [&]() __attribute__((always_inline)) -> bool { return !__all(((badtag(qg[0], tag) | badtag(qg[1], tag)) & 0xffffu) == 0); };
#define STEP(m) if constexpr ((m) >= D0 + 5 && (m) < QPOS) { \
      if constexpr ((m) == GPOS) gather(); \
      WAITVM(DEPTH - 1 + (((m) >= GPOS && (m) - DEPTH < GPOS) ? 2 : 0)); \
      stream(m); }
#define STEP4(m) STEP(m) STEP(m + 1) STEP(m + 2) STEP(m + 3)
    STEP4(4) STEP4(8) STEP4(12) STEP4(16) STEP4(20) STEP4(24) STEP4(28)
#define LATE(k) if constexpr (QPOS + (k) < 32) { SB(); stream(QPOS + (k)); }
    STAMP(4);
    if (gather_bad()) {
#pragma unroll 1
      for (unsigned spin = 0; spin < SPIN_CAP; ++spin) { gather(); if (!gather_bad()) break; }
    }
    {
      const v4f a = unpack4(qg[0]) + unpack4(qg[1]);
      const float e0 = fold32(a[0], a[2]), e1 = fold32(a[1], a[3]);
      *(LAS unsigned*)(lds + L_Q + w * 256 + 8 * (l & 31) + 4 * (l >> 5)) = pkbf(e0, e1);
    }
    LATE(0)
    lds_barrier();
    STAMP(5);

    const v4u qf = *(LAS v4u*)(lds + a_qf);
    float sc[16];
    {
      v4f dq[16];
#pragma unroll
      for (int i = 0; i < 16; ++i) { dq[i] = mfma16(kk[i], qf, v4f{0.f, 0.f, 0.f, 0.f}); if (i == 7) { LATE(1) } }
      LATE(2)
#pragma unroll
      for (int i = 0; i < 16; ++i) sc[i] = dq[i][0] + dpp<SHL4>(dq[i][1]);
      LATE(3)
#pragma unroll
      for (int i = 0; i < 16; ++i) sc[i] += dpp<SHL8>(dq[i][2]);
      LATE(4)
#pragma unroll
      for (int i = 0; i < 16; ++i) { sc[i] += dpp<SHL12>(dq[i][3]); asm("" : "+v"(sc[i])); }
      LATE(5)
    }
    float mx = sc[0];
#pragma unroll
    for (int i = 1; i < 16; ++i) mx = vmax(mx, sc[i]);
    LATE(6)
    mx = foldmax16(mx, mx);
    mx = foldmax32(mx, mx);
    if (l < 4) *(LAS float*)(lds + L_MAX + (w * 4 + l) * 4) = mx;
    LATE(7) LATE(8) LATE(9) LATE(10) LATE(11) LATE(12) LATE(13) LATE(14) LATE(15)
    lds_barrier();
    constexpr float CS = 0.08838834764831845f * 1.4426950408889634f;
    const LAS float* pm = (const LAS float*)(lds + L_MAX) + (l & 3);
    const float M2 = vmax(vmax(pm[0], pm[4]), vmax(pm[8], pm[12])) * CS;
#pragma unroll
    for (int i = 0; i < 16; i += 2) {
      const v2f t = v2f{sc[i], sc[i + 1]} * CS - M2;
      sc[i] = __builtin_amdgcn_exp2f(t[0]); sc[i + 1] = __builtin_amdgcn_exp2f(t[1]);
    }
    v4u pf[2][4];
#pragma unroll
    for (int u = 0; u < 2; ++u)
#pragma unroll
      for (int m = 0; m < 4; ++m) pf[u][0][m] = sel(m_b0, pkbf(sc[8 * u + 2 * m], sc[8 * u + 2 * m + 1]), 0u);
    const v4u ones = {0x3f803f80u, 0x3f803f80u, 0x3f803f80u, 0x3f803f80u};
    const v4f lsum = mfma16(pf[1][0], ones, mfma16(pf[0][0], ones, v4f{0.f, 0.f, 0.f, 0.f}));
#pragma unroll
    for (int u = 0; u < 2; ++u)
#pragma unroll
      for (int m = 0; m < 4; ++m) {
        pf[u][1][m] = U(dpp<SHR4>(F(pf[u][0][m])));
        pf[u][2][m] = U(dpp<SHR8>(F(pf[u][0][m])));
        pf[u][3][m] = U(dpp<SHR12>(F(pf[u][0][m])));
      }
    if (l == 0) *(LAS v4f*)(lds + L_SUM + 16 * w) = lsum;
    const unsigned Mw = __builtin_amdgcn_readlane(U(M2), w);
    STAMP(6);

    v4f acc[2] = {v4f{0.f, 0.f, 0.f, 0.f}, v4f{0.f, 0.f, 0.f, 0.f}};
#pragma unroll
    for (int u = 0; u < 2; ++u) {
      v4u vt[8];
#pragma unroll
      for (int m = 0; m < 4; ++m) {
#pragma unroll
        for (int d = 0; d < 8; ++d) vt[d][m] = __builtin_amdgcn_perm(vv[8 * u + 2 * m + 1][d >> 1], vv[8 * u + 2 * m][d >> 1], (d & 1) ? 0x07060302u : 0x05040100u);
        if (m < 3) SB();
      }
#pragma unroll
      for (int d = 0; d < 8; ++d) acc[d & 1] = mfma16(pf[u][d >> 1], vt[d], acc[d & 1]);
      SB();
    }
    STAMP(7);
#pragma unroll
    for (int h = 0; h < 4; ++h) *(LAS v2f*)(lds + a_mw + h * 512) = v2f{acc[0][h], acc[1][h]};
    lds_barrier();
    {
      const LAS float* pl = (const LAS float*)(lds + a_ls);
      const v4f ps = {pl[0], pl[4], pl[8], pl[12]};
      const LAS v4f* src = (const LAS v4f*)(lds + a_mr);
      const v4f o0 = src[0], o1 = src[128], o2 = src[256], o3 = src[384];
      SB();
      const v4u pk = pack4<0u>((o0 + o1) + (o2 + o3), tag);
      __builtin_amdgcn_raw_buffer_store_b128(v4u{sel(m_ml, Mw, pk[0]), pk[1], sel(m_ml, U((ps[0] + ps[1]) + (ps[2] + ps[3])), pk[2]), pk[3]}, rws, 16 * l, s_op, AUX_SC1);
    }
    STAMP(8);
    WAITVM(0);
    STAMP(14);
  } else {
    SB();
    __builtin_amdgcn_s_barrier();
    SB();
    tag = *(const CAS unsigned*)(ka[6] + WS_EPOCH) + 1u;
    tail_addresses();
    __builtin_amdgcn_s_barrier();
    lds_barrier();
    if (PUBW == 4 && w == 4 && l < 32)
      __builtin_amdgcn_raw_buffer_store_b128(*(LAS v4u*)(lds + L_MRG + 16 * l), rws, (l >> 4) * 8192 + 16 * (l & 15), WS_QP + (gn * 8 + rq) * 2048 + 256 * cq, AUX_SC1);
    lds_barrier();
    if (WOAT == 3) issue_wo();
    __builtin_amdgcn_s_barrier();
    unsigned t0 = __builtin_amdgcn_raw_buffer_load_b32(rws, (l & 3) * 128, WS_FP + ((c * 64 + gn * 8 + r8) * 128 + 32 * (w - 4)) * 16, 0);
    unsigned t1 = __builtin_amdgcn_raw_buffer_load_b32(__builtin_amdgcn_make_buffer_rsrc((void*)ka[5], 0, (w == 4 && j < 8) ? 2048 : 0, RSRC_FLAGS), (l & 1) * 1024, (g * 8 + j) * 16, 0);
    unsigned t2 = __builtin_amdgcn_raw_buffer_load_b32(__builtin_amdgcn_make_buffer_rsrc((void*)ka[4], 0, (w == 5 && WOAT >= 5) ? 4096 * 512 * 2 : 0, RSRC_FLAGS), 0, wo_s, AUX_NT);
    SB();
    if (WOAT == 4) issue_wo();
    asm volatile("" :: "v"(t0), "v"(t1), "v"(t2));
    SB();
    __builtin_amdgcn_s_barrier();
    if (WOAT == 5) issue_wo();
  }

  __builtin_amdgcn_s_barrier();
  SB();
  v4u wf[2];
  {
    v4u d[2];
    auto poll = [&]() __attribute__((always_inline)) {
      MEMBAR();
#pragma unroll
      for (int i = 0; i < 2; ++i) d[i] = __builtin_amdgcn_raw_buffer_load_b128(rws, v_h1, s_h1 + i * 32768, AUX_SC1);
      SB();
      COUNT(rounds1);
    };
    auto poll_bad = [&]() __attribute__((always_inline)) -> bool { return !__all(((badtag(d[0], tag) | badtag(d[1], tag)) & 0xffffu) == 0); };
    if (WOAT == 6 && w >= 4) issue_wo();
    poll();
    if (WOAT == 7 && w >= 4) issue_wo();
    asm volatile("s_waitcnt vmcnt(2)" ::: "memory");
    STAMP(15);
    lds_barrier();
#pragma unroll
    for (int ks = 0; ks < 2; ++ks) wf[ks] = tr_frag(lds + a_tb + ks * 32 * 256, 4 * 256);
    SB();
    STAMP(9);
    if (poll_bad()) {
#pragma unroll 1
      for (unsigned spin = 0; spin < SPIN_CAP; ++spin) { poll(); if (!poll_bad()) break; }
    }
    STAMP(10);
    const float m0 = dpp<SHL4>(F(d[0][0])), m1 = dpp<SHL4>(F(d[1][0])), l0 = dpp<SHL4>(F(d[0][2])), l1 = dpp<SHL4>(F(d[1][2]));
    float Mx = vmax(m0, m1);
    Mx = foldmax32(Mx, Mx);
    Mx = foldmax16(Mx, Mx);
    Mx = vmax(Mx, dpp<ROR8>(Mx));
    const float w0 = __builtin_amdgcn_exp2f(m0 - Mx), w1 = __builtin_amdgcn_exp2f(m1 - Mx);
    const v4f a = unpack4(d[0]) * w0 + unpack4(d[1]) * w1;
    float el = l0 * w0 + l1 * w1;
    const float t0 = fold32(a[0], a[2]), t1 = fold32(a[1], a[3]);
    el = fold32(el, el);
    float e = fold16(t0, t1);
    el = fold16(el, el);
    e += dpp<ROR8>(e);
    el += dpp<ROR8>(el);
    e *= __builtin_amdgcn_rcpf(el);
    if (!(l & 12)) *(LAS unsigned short*)(lds + a_ow) = (unsigned short)pkbf(e, e);
  }
  lds_barrier();
  {
    const v4u a0 = *(const LAS v4u*)(lds + a_or), a1 = *(const LAS v4u*)(lds + a_or + 64);
    const v4f f = mfma16(a1, wf[1], mfma16(a0, wf[0], v4f{0.f, 0.f, 0.f, 0.f}));
    if (l < 16) __builtin_amdgcn_raw_buffer_store_b128(v4u{U(f[0]), tag, U(f[1]), tag}, rws, 16 * l, s_fp, AUX_SC1);
  }
  STAMP(11);
  if (j < 8) {
    const int f = g * 8 + j, fc = f >> 4, col0 = 8 * (f & 15);
    const unsigned vo = (l >> 3) * 2048 + 16 * (l & 7);
    const unsigned so = WS_FP + ((fc * 64 + 8 * w) * 128 + col0) * 16;
    v4u d;
    WAITVM(0);
#pragma unroll 1
    for (unsigned spin = 0; spin < SPIN_CAP; ++spin) {
      MEMBAR();
      d = __builtin_amdgcn_raw_buffer_load_b128(rws, vo, so, AUX_SC1);
      COUNT(rounds2);
      if (__all(badtag(d, tag) == 0)) break;
    }
    STAMP(12);
    float r = fold32(F(d[0]), F(d[2]));
    r = fold16(r, r);
    r += dpp<ROR8>(r);
    *(LAS float*)(lds + L_MRG + ((l >> 5) * 8 + (l & 7)) * 32 + 4 * w) = r;
    lds_barrier();
    if (w == 0 && l < 16) {
      const v4f t = *(LAS v4f*)(lds + L_MRG + 32 * l) + *(LAS v4f*)(lds + L_MRG + 32 * l + 16);
      const float z = (t[0] + t[1]) + (t[2] + t[3]);
      ((GAS unsigned short*)ka[5])[(l >> 3) * 512 + 128 * fc + col0 + (l & 7)] = (unsigned short)pkbf(z, z);
      if (f == 0 && l == 0) __builtin_amdgcn_raw_buffer_store_b32(tag, rws, 0, WS_EPOCH, AUX_SC1);
    }
    STAMP(13);
  }
  DUMP();
}

extern "C" void kernel_launch(void* const* d_in, const int* in_sizes, int n_in, void* d_out, int out_size, void* d_ws, size_t ws_size,
                              hipStream_t stream) {
  (void)in_sizes; (void)n_in; (void)out_size; (void)ws_size;
  decode_fused<<<256, 512, 0, stream>>>(d_in[0], d_in[1], d_in[2], d_in[3], d_in[4], d_out, d_ws, 0ull);
}
```

```cpp
#define PROBE 0
#define PROBED 1
#include <hip/hip_runtime.h>
#include <hip/hip_bf16.h>

typedef unsigned v4u __attribute__((ext_vector_type(4)));
typedef unsigned v2u __attribute__((ext_vector_type(2)));
typedef float v4f __attribute__((ext_vector_type(4)));
typedef float v2f __attribute__((ext_vector_type(2)));
typedef short v4s __attribute__((ext_vector_type(4)));
typedef __bf16 v8bf __attribute__((ext_vector_type(8)));
typedef __bf16 v2bf __attribute__((ext_vector_type(2)));
typedef unsigned long long v8q __attribute__((ext_vector_type(8)));
#define LAS __attribute__((address_space(3)))
#define GAS __attribute__((address_space(1)))
#define CAS __attribute__((address_space(4)))
#define SB() __builtin_amdgcn_sched_barrier(0)
#define MEMBAR() asm volatile("" ::: "memory")
#define WAITVM(n) do { SB(); __builtin_amdgcn_s_waitcnt((((n) & 15) | (((n) >> 4) << 14) | (7 << 4) | (15 << 8))); SB(); } while (0)

#define DEPTH 8
#define D0 4
#define GPOS 18
#define QPOS 24
#define WOAT 5
#define NOB7 0
#define PUBW 4
constexpr int SK = 32768, WIN = 4096, DH = 128;
constexpr int AUX_NT = 2, AUX_SC1 = 16;
constexpr unsigned RSRC_FLAGS = 0x00020000u;
constexpr unsigned WS_EPOCH = 0;
constexpr unsigned WS_QP = 4096;
constexpr unsigned WS_OP = WS_QP + 8 * 2 * 4 * 2048;
constexpr unsigned WS_FP = WS_OP + 8 * 2 * 16 * 4096;
constexpr unsigned WS_END = WS_FP + 4 * 64 * 128 * 16;
constexpr unsigned WS_STAMP = 4u << 20;
constexpr int L_W = 0;
constexpr int L_MRG = 16384;
constexpr int L_Q = L_MRG + 4 * 4 * 128 * 4;
constexpr int L_MAX = L_Q + 1024;
constexpr int L_SUM = L_MAX + 64;
constexpr int L_O = L_SUM + 64;
constexpr int L_X = L_O + 256;
constexpr int L_END = L_X + 512;
constexpr unsigned SPIN_CAP = 1u << 18;

__device__ __forceinline__ v4f mfma16(v4u a, v4u b, v4f c) {
  return __builtin_amdgcn_mfma_f32_16x16x32_bf16(__builtin_bit_cast(v8bf, a), __builtin_bit_cast(v8bf, b), c, 0, 0, 0);
}
__device__ __forceinline__ unsigned pkbf(float lo, float hi) {
  v2bf r; r[0] = (__bf16)lo; r[1] = (__bf16)hi; return __builtin_bit_cast(unsigned, r);
}
template <int CTRL> __device__ __forceinline__ float dpp(float x) {
  return __builtin_bit_cast(float, __builtin_amdgcn_mov_dpp(__builtin_bit_cast(int, x), CTRL, 0xf, 0xf, true));
}
constexpr int SHL4 = 0x104, SHL8 = 0x108, SHL12 = 0x10c, SHR4 = 0x114, SHR8 = 0x118, SHR12 = 0x11c, ROR8 = 0x128;
#define F(x) __uint_as_float(x)
#define U(x) __float_as_uint(x)
__device__ __forceinline__ float vmax(float a, float b) { return __builtin_elementwise_maximum(a, b); }
__device__ __forceinline__ float fold32(float a, float b) { auto t = __builtin_amdgcn_permlane32_swap(U(a), U(b), false, false); return F(t[0]) + F(t[1]); }
__device__ __forceinline__ float fold16(float a, float b) { auto t = __builtin_amdgcn_permlane16_swap(U(a), U(b), false, false); return F(t[0]) + F(t[1]); }
__device__ __forceinline__ float foldmax32(float a, float b) { auto t = __builtin_amdgcn_permlane32_swap(U(a), U(b), false, false); return vmax(F(t[0]), F(t[1])); }
__device__ __forceinline__ float foldmax16(float a, float b) { auto t = __builtin_amdgcn_permlane16_swap(U(a), U(b), false, false); return vmax(F(t[0]), F(t[1])); }
__device__ __forceinline__ v4u tr_frag(LAS unsigned char* p, int second_off) {
  const v4s a = __builtin_amdgcn_ds_read_tr16_b64_v4i16((LAS v4s*)p);
  const v4s b = __builtin_amdgcn_ds_read_tr16_b64_v4i16((LAS v4s*)(p + second_off));
  const v2u a2 = __builtin_bit_cast(v2u, a), b2 = __builtin_bit_cast(v2u, b);
  return v4u{a2[0], a2[1], b2[0], b2[1]};
}
__device__ __forceinline__ void lds_barrier() {
  SB();
  asm volatile("s_waitcnt lgkmcnt(0)" ::: "memory");
  __builtin_amdgcn_s_barrier();
  MEMBAR();
  SB();
}
__device__ __forceinline__ unsigned badtag(v4u d, unsigned tag) { return (d[1] ^ tag) | (d[3] ^ tag); }
template <unsigned RND>
__device__ __forceinline__ v4u pack4(v4f a, unsigned tag) {
  const unsigned r0 = U(a[0]) + RND, r1 = U(a[1]) + RND, r2 = U(a[2]) + RND, r3 = U(a[3]) + RND;
  return v4u{__builtin_amdgcn_perm(r0, r1, 0x07060503u), __builtin_amdgcn_perm(r1, tag, 0x06050100u),
             __builtin_amdgcn_perm(r2, r3, 0x07060503u), __builtin_amdgcn_perm(r3, tag, 0x06050100u)};
}
__device__ __forceinline__ v4f unpack4(v4u d) {
  return v4f{F(d[0] & 0xffffff00u), F(__builtin_amdgcn_perm(d[0], d[1], 0x0403020cu)), F(d[2] & 0xffffff00u), F(__builtin_amdgcn_perm(d[2], d[3], 0x0403020cu))};
}
__device__ __forceinline__ unsigned sel(unsigned long long m, unsigned t, unsigned f) { return __builtin_amdgcn_inverse_ballot_w64(m) ? t : f; }
#define STAMP(i) do { SB(); ts[i] = __builtin_amdgcn_s_memrealtime(); SB(); } while (0)
#define COUNT(x) ++x
#define DSTAMP(k) do { SB(); td[k] = (unsigned)__builtin_amdgcn_s_memrealtime(); SB(); } while (0)
#define DUMP() do { if (td[PROBED]) { const unsigned long long e = __builtin_amdgcn_s_memrealtime() + 16 * (td[PROBED] - (unsigned)ts[0]); while (__builtin_amdgcn_s_memrealtime() < e) __builtin_amdgcn_s_sleep(2); } } while (0)

__global__ void __launch_bounds__(512, 2)
decode_fused(const void* x_, const void* kc_, const void* vc_, const void* wq_, const void* wo_, void* out_, void* ws_, unsigned long long pad_) {
  __shared__ __attribute__((aligned(16))) unsigned char lds_raw[L_END];
  LAS unsigned char* lds = (LAS unsigned char*)lds_raw;
  unsigned long long ts[16] = {0, 0, 0, 0, 0, 0, 0, 0, 0, 0, 0, 0, 0, 0, 0, 0};
  unsigned rounds0 = 0, rounds1 = 0, rounds2 = 0;
  unsigned td[8] = {0, 0, 0, 0, 0, 0, 0, 0};
  STAMP(0);
  v8q ka;
  asm volatile("s_load_dwordx16 %0, %1, 0x0" : "=s"(ka) : "s"(__builtin_amdgcn_kernarg_segment_ptr()));
  SB();
  const int bid = blockIdx.x, g = bid & 7, j = bid >> 3, b = j >> 4, s = j & 15;
  const int w = __builtin_amdgcn_readfirstlane(threadIdx.x >> 6), l = threadIdx.x & 63;
  const int g4 = l >> 4, bank = (l >> 2) & 3;
  const int gn = (g + 1) & 7;
  const int rq = j >> 3, cq = j & 7;
  const int r8 = j >> 2, hh = r8 >> 1, half = r8 & 1, c = j & 3;
  unsigned wq_v = (l >> 3) * 8192 + 16 * (l & 7), wq_s = (128 * rq + 32 * w) * 8192 + gn * 1024 + 128 * cq;
  unsigned x_v = (l & 4) * 256 + 16 * (l & 3), x_s = 256 * rq + 64 * w;
  unsigned koff = 256 * bank + 64 * g4 + 16 * (l & 3);
  unsigned long long kvoff = ((unsigned long long)(b * 8 + g) * SK + (SK - WIN) + s * 256) * DH * 2;
  asm volatile("" : "+v"(wq_v), "+v"(x_v), "+v"(koff));
  asm volatile("" : "+s"(wq_s), "+s"(x_s), "+s"(kvoff));
  SB();
  asm volatile("s_waitcnt lgkmcnt(0)" : "+s"(ka));
  DSTAMP(0);
  const __amdgpu_buffer_rsrc_t rws = __builtin_amdgcn_make_buffer_rsrc((void*)ka[6], 0, WS_END, RSRC_FLAGS);
  unsigned tag;
  unsigned v_h1, s_h1, a_ow, a_or, a_tb, s_fp;
  auto tail_addresses = [&]() __attribute__((always_inline)) {
    v_h1 = (l >> 3) * 4096 + 16 * (l & 7);
    s_h1 = WS_OP + ((gn * 2 + (w >> 2)) * 16 * 4 + hh) * 1024 + (4 * half + (w & 3)) * 128;
    a_ow = L_O + (w >> 2) * 128 + (16 * (w & 3) + 4 * (l & 3) + 2 * (l >> 5) + (g4 & 1)) * 2;
    a_or = L_O + (l & 1) * 128 + 16 * g4;
    a_tb = L_W + (8 * g4 + bank) * 256 + 8 * (l & 3) + 32 * w;
    s_fp = WS_FP + ((c * 64 + gn * 8 + r8) * 128 + 16 * w) * 16;
    asm volatile("" : "+v"(v_h1), "+v"(a_ow), "+v"(a_or), "+v"(a_tb));
    asm volatile("" : "+s"(s_h1), "+s"(s_fp));
  };

  const unsigned wo_s = (gn * 512 + 64 * r8 + 16 * (w & 3)) * 1024 + 256 * c;
  auto issue_wo = [&]() __attribute__((always_inline)) {
    const __amdgpu_buffer_rsrc_t ro = __builtin_amdgcn_make_buffer_rsrc((void*)ka[4], 0, 4096 * 512 * 2, RSRC_FLAGS);
#pragma unroll
    for (int i = 0; i < 4; ++i)
      __builtin_amdgcn_raw_ptr_buffer_load_lds(ro, lds + L_W + (w & 3) * 4096 + i * 1024, 16, g4 * 1024 + 16 * (l & 15), wo_s + i * 4096, 0, AUX_NT);
    SB();
  };
  if (__builtin_expect(w < 4, 1)) {
    const __amdgpu_buffer_rsrc_t rq_ = __builtin_amdgcn_make_buffer_rsrc((void*)ka[3], 0, 512 * 4096 * 2, RSRC_FLAGS);
    const __amdgpu_buffer_rsrc_t rx = __builtin_amdgcn_make_buffer_rsrc((void*)ka[0], 0, 2 * 512 * 2, RSRC_FLAGS);
    v4u wv[4], kk[16], vv[16];
#pragma unroll
    for (int i = 0; i < 4; ++i) { wv[i] = __builtin_amdgcn_raw_buffer_load_b128(rq_, wq_v, wq_s + i * 65536, AUX_NT); SB(); }
    DSTAMP(1);
    const v4u xv = __builtin_amdgcn_raw_buffer_load_b128(rx, x_v, x_s, 0);
    SB();
    DSTAMP(2);
    const __amdgpu_buffer_rsrc_t rk = __builtin_amdgcn_make_buffer_rsrc((void*)(ka[1] + kvoff), 0, 65536, RSRC_FLAGS);
    const __amdgpu_buffer_rsrc_t rv = __builtin_amdgcn_make_buffer_rsrc((void*)(ka[2] + kvoff), 0, 65536, RSRC_FLAGS);
    auto stream = [&](int m) __attribute__((always_inline)) {
      if (m < 16) kk[m] = __builtin_amdgcn_raw_buffer_load_b128(rk, koff, (((m + s) & 15) * 4 + w) * 1024, AUX_NT);
      else vv[m - 16] = __builtin_amdgcn_raw_buffer_load_b128(rv, 16 * l, (((m + s) & 15) * 4 + w) * 1024, AUX_NT);
      SB();
    };
    __builtin_amdgcn_s_barrier();
    SB();
    DSTAMP(3);
#pragma unroll
    for (int m = 0; m < D0; ++m) stream(m);
    DSTAMP(4);
    tag = *(const CAS unsigned*)(ka[6] + WS_EPOCH) + 1u;
    STAMP(1);

#pragma unroll
    for (int i = 0; i < 4; ++i) { *(LAS v4u*)(lds + L_W + (4 * w + i) * 1024 + 16 * l) = wv[i]; SB(); if (i == 0) DSTAMP(5); if (i == 3) DSTAMP(6); stream(D0 + i); }
    *(LAS v4u*)(lds + L_X + (l & 4) * 64 + 64 * w + 16 * (l & 3)) = xv;
    SB();
    stream(D0 + 4);
    STAMP(2);
    lds_barrier();
    {
      LAS unsigned char* tb = lds + L_W + (8 * g4 + bank) * 128 + 8 * (l & 3) + 32 * w;
      v4u f[4], xa[4];
#pragma unroll
      for (int ks = 0; ks < 4; ++ks) { f[ks] = tr_frag(tb + ks * 32 * 128, 4 * 128); xa[ks] = *(LAS v4u*)(lds + L_X + (l & 1) * 256 + 64 * ks + 16 * g4); }
      SB();
      v4f qa = mfma16(f[0], xa[0], v4f{0.f, 0.f, 0.f, 0.f}), qb = mfma16(f[1], xa[1], v4f{0.f, 0.f, 0.f, 0.f});
      qa = mfma16(f[2], xa[2], qa); qb = mfma16(f[3], xa[3], qb);
      if ((l & 15) < 2) *(LAS v4u*)(lds + L_MRG + (l & 1) * 256 + 64 * w + 16 * g4) = pack4<0x80u>(qa + qb, tag);
      lds_barrier();
      if (PUBW == 0 && w == 0 && l < 32)
        __builtin_amdgcn_raw_buffer_store_b128(*(LAS v4u*)(lds + L_MRG + 16 * l), rws, (l >> 4) * 8192 + 16 * (l & 15), WS_QP + (gn * 8 + rq) * 2048 + 256 * cq, AUX_SC1);
      SB();
    }
    STAMP(3);
    tail_addresses();
    unsigned a_qf = L_Q + (l & 3) * 256 + 16 * (4 * g4 + bank);
    unsigned a_mw = L_MRG + (w * 4 * 128 + 8 * (l & 15) + 2 * g4) * 4, a_mr = L_MRG + (w * 128 + 16 * (l >> 3) + 4 * (l & 3)) * 4, a_ls = L_SUM + 4 * w;
    unsigned s_op = WS_OP + (((g * 2 + b) * 16 + s) * 4 + w) * 1024;
    unsigned long long m_b0 = __ballot(bank == 0), m_ml = __ballot(l & 4);
    asm volatile("" : "+v"(a_qf), "+v"(a_mw), "+v"(a_mr), "+v"(a_ls));
    asm volatile("" : "+s"(s_op), "+s"(m_b0), "+s"(m_ml));

    v4u qg[2];
    auto gather = [&]() __attribute__((always_inline)) {
      MEMBAR();
#pragma unroll
      for (int i = 0; i < 2; ++i)
        qg[i] = __builtin_amdgcn_raw_buffer_load_b128(rws, (l >> 5) * 2048 + 16 * (l & 31), WS_QP + (g * 2 + b) * 8192 + i * 4096 + w * 512, AUX_SC1);
      SB();
      COUNT(rounds0);
    };
    auto gather_bad = [&]() __attribute__((always_inline)) -> bool { return !__all(((badtag(qg[0], tag) | badtag(qg[1], tag)) & 0xffffu) == 0); };
#define STEP(m) if constexpr ((m) >= D0 + 5 && (m) < QPOS) { \
      if constexpr ((m) == GPOS) gather(); \
      WAITVM(DEPTH - 1 + (((m) >= GPOS && (m) - DEPTH < GPOS) ? 2 : 0)); \
      stream(m); }
#define STEP4(m) STEP(m) STEP(m + 1) STEP(m + 2) STEP(m + 3)
    STEP4(4) STEP4(8) STEP4(12) STEP4(16) STEP4(20) STEP4(24) STEP4(28)
#define LATE(k) if constexpr (QPOS + (k) < 32) { SB(); stream(QPOS + (k)); }
    STAMP(4);
    if (gather_bad()) {
#pragma unroll 1
      for (unsigned spin = 0; spin < SPIN_CAP; ++spin) { gather(); if (!gather_bad()) break; }
    }
    {
      const v4f a = unpack4(qg[0]) + unpack4(qg[1]);
      const float e0 = fold32(a[0], a[2]), e1 = fold32(a[1], a[3]);
      *(LAS unsigned*)(lds + L_Q + w * 256 + 8 * (l & 31) + 4 * (l >> 5)) = pkbf(e0, e1);
    }
    LATE(0)
    lds_barrier();
    STAMP(5);

    const v4u qf = *(LAS v4u*)(lds + a_qf);
    float sc[16];
    {
      v4f dq[16];
#pragma unroll
      for (int i = 0; i < 16; ++i) { dq[i] = mfma16(kk[i], qf, v4f{0.f, 0.f, 0.f, 0.f}); if (i == 7) { LATE(1) } }
      LATE(2)
#pragma unroll
      for (int i = 0; i < 16; ++i) sc[i] = dq[i][0] + dpp<SHL4>(dq[i][1]);
      LATE(3)
#pragma unroll
      for (int i = 0; i < 16; ++i) sc[i] += dpp<SHL8>(dq[i][2]);
      LATE(4)
#pragma unroll
      for (int i = 0; i < 16; ++i) { sc[i] += dpp<SHL12>(dq[i][3]); asm("" : "+v"(sc[i])); }
      LATE(5)
    }
    float mx = sc[0];
#pragma unroll
    for (int i = 1; i < 16; ++i) mx = vmax(mx, sc[i]);
    LATE(6)
    mx = foldmax16(mx, mx);
    mx = foldmax32(mx, mx);
    if (l < 4) *(LAS float*)(lds + L_MAX + (w * 4 + l) * 4) = mx;
    LATE(7) LATE(8) LATE(9) LATE(10) LATE(11) LATE(12) LATE(13) LATE(14) LATE(15)
    lds_barrier();
    constexpr float CS = 0.08838834764831845f * 1.4426950408889634f;
    const LAS float* pm = (const LAS float*)(lds + L_MAX) + (l & 3);
    const float M2 = vmax(vmax(pm[0], pm[4]), vmax(pm[8], pm[12])) * CS;
#pragma unroll
    for (int i = 0; i < 16; i += 2) {
      const v2f t = v2f{sc[i], sc[i + 1]} * CS - M2;
      sc[i] = __builtin_amdgcn_exp2f(t[0]); sc[i + 1] = __builtin_amdgcn_exp2f(t[1]);
    }
    v4u pf[2][4];
#pragma unroll
    for (int u = 0; u < 2; ++u)
#pragma unroll
      for (int m = 0; m < 4; ++m) pf[u][0][m] = sel(m_b0, pkbf(sc[8 * u + 2 * m], sc[8 * u + 2 * m + 1]), 0u);
    const v4u ones = {0x3f803f80u, 0x3f803f80u, 0x3f803f80u, 0x3f803f80u};
    const v4f lsum = mfma16(pf[1][0], ones, mfma16(pf[0][0], ones, v4f{0.f, 0.f, 0.f, 0.f}));
#pragma unroll
    for (int u = 0; u < 2; ++u)
#pragma unroll
      for (int m = 0; m < 4; ++m) {
        pf[u][1][m] = U(dpp<SHR4>(F(pf[u][0][m])));
        pf[u][2][m] = U(dpp<SHR8>(F(pf[u][0][m])));
        pf[u][3][m] = U(dpp<SHR12>(F(pf[u][0][m])));
      }
    if (l == 0) *(LAS v4f*)(lds + L_SUM + 16 * w) = lsum;
    const unsigned Mw = __builtin_amdgcn_readlane(U(M2), w);
    STAMP(6);

    v4f acc[2] = {v4f{0.f, 0.f, 0.f, 0.f}, v4f{0.f, 0.f, 0.f, 0.f}};
#pragma unroll
    for (int u = 0; u < 2; ++u) {
      v4u vt[8];
#pragma unroll
      for (int m = 0; m < 4; ++m) {
#pragma unroll
        for (int d = 0; d < 8; ++d) vt[d][m] = __builtin_amdgcn_perm(vv[8 * u + 2 * m + 1][d >> 1], vv[8 * u + 2 * m][d >> 1], (d & 1) ? 0x07060302u : 0x05040100u);
        if (m < 3) SB();
      }
#pragma unroll
      for (int d = 0; d < 8; ++d) acc[d & 1] = mfma16(pf[u][d >> 1], vt[d], acc[d & 1]);
      SB();
    }
    STAMP(7);
#pragma unroll
    for (int h = 0; h < 4; ++h) *(LAS v2f*)(lds + a_mw + h * 512) = v2f{acc[0][h], acc[1][h]};
    lds_barrier();
    {
      const LAS float* pl = (const LAS float*)(lds + a_ls);
      const v4f ps = {pl[0], pl[4], pl[8], pl[12]};
      const LAS v4f* src = (const LAS v4f*)(lds + a_mr);
      const v4f o0 = src[0], o1 = src[128], o2 = src[256], o3 = src[384];
      SB();
      const v4u pk = pack4<0u>((o0 + o1) + (o2 + o3), tag);
      __builtin_amdgcn_raw_buffer_store_b128(v4u{sel(m_ml, Mw, pk[0]), pk[1], sel(m_ml, U((ps[0] + ps[1]) + (ps[2] + ps[3])), pk[2]), pk[3]}, rws, 16 * l, s_op, AUX_SC1);
    }
    STAMP(8);
    WAITVM(0);
    STAMP(14);
  } else {
    SB();
    __builtin_amdgcn_s_barrier();
    SB();
    tag = *(const CAS unsigned*)(ka[6] + WS_EPOCH) + 1u;
    tail_addresses();
    __builtin_amdgcn_s_barrier();
    lds_barrier();
    if (PUBW == 4 && w == 4 && l < 32)
      __builtin_amdgcn_raw_buffer_store_b128(*(LAS v4u*)(lds + L_MRG + 16 * l), rws, (l >> 4) * 8192 + 16 * (l & 15), WS_QP + (gn * 8 + rq) * 2048 + 256 * cq, AUX_SC1);
    lds_barrier();
    if (WOAT == 3) issue_wo();
    __builtin_amdgcn_s_barrier();
    unsigned t0 = __builtin_amdgcn_raw_buffer_load_b32(rws, (l & 3) * 128, WS_FP + ((c * 64 + gn * 8 + r8) * 128 + 32 * (w - 4)) * 16, 0);
    unsigned t1 = __builtin_amdgcn_raw_buffer_load_b32(__builtin_amdgcn_make_buffer_rsrc((void*)ka[5], 0, (w == 4 && j < 8) ? 2048 : 0, RSRC_FLAGS), (l & 1) * 1024, (g * 8 + j) * 16, 0);
    unsigned t2 = __builtin_amdgcn_raw_buffer_load_b32(__builtin_amdgcn_make_buffer_rsrc((void*)ka[4], 0, (w == 5 && WOAT >= 5) ? 4096 * 512 * 2 : 0, RSRC_FLAGS), 0, wo_s, AUX_NT);
    SB();
    if (WOAT == 4) issue_wo();
    asm volatile("" :: "v"(t0), "v"(t1), "v"(t2));
    SB();
    __builtin_amdgcn_s_barrier();
    if (WOAT == 5) issue_wo();
  }

  __builtin_amdgcn_s_barrier();
  SB();
  v4u wf[2];
  {
    v4u d[2];
    auto poll = [&]() __attribute__((always_inline)) {
      MEMBAR();
#pragma unroll
      for (int i = 0; i < 2; ++i) d[i] = __builtin_amdgcn_raw_buffer_load_b128(rws, v_h1, s_h1 + i * 32768, AUX_SC1);
      SB();
      COUNT(rounds1);
    };
    auto poll_bad = [&]() __attribute__((always_inline)) -> bool { return !__all(((badtag(d[0], tag) | badtag(d[1], tag)) & 0xffffu) == 0); };
    if (WOAT == 6 && w >= 4) issue_wo();
    poll();
    if (WOAT == 7 && w >= 4) issue_wo();
    asm volatile("s_waitcnt vmcnt(2)" ::: "memory");
    STAMP(15);
    lds_barrier();
#pragma unroll
    for (int ks = 0; ks < 2; ++ks) wf[ks] = tr_frag(lds + a_tb + ks * 32 * 256, 4 * 256);
    SB();
    STAMP(9);
    if (poll_bad()) {
#pragma unroll 1
      for (unsigned spin = 0; spin < SPIN_CAP; ++spin) { poll(); if (!poll_bad()) break; }
    }
    STAMP(10);
    const float m0 = dpp<SHL4>(F(d[0][0])), m1 = dpp<SHL4>(F(d[1][0])), l0 = dpp<SHL4>(F(d[0][2])), l1 = dpp<SHL4>(F(d[1][2]));
    float Mx = vmax(m0, m1);
    Mx = foldmax32(Mx, Mx);
    Mx = foldmax16(Mx, Mx);
    Mx = vmax(Mx, dpp<ROR8>(Mx));
    const float w0 = __builtin_amdgcn_exp2f(m0 - Mx), w1 = __builtin_amdgcn_exp2f(m1 - Mx);
    const v4f a = unpack4(d[0]) * w0 + unpack4(d[1]) * w1;
    float el = l0 * w0 + l1 * w1;
    const float t0 = fold32(a[0], a[2]), t1 = fold32(a[1], a[3]);
    el = fold32(el, el);
    float e = fold16(t0, t1);
    el = fold16(el, el);
    e += dpp<ROR8>(e);
    el += dpp<ROR8>(el);
    e *= __builtin_amdgcn_rcpf(el);
    if (!(l & 12)) *(LAS unsigned short*)(lds + a_ow) = (unsigned short)pkbf(e, e);
  }
  lds_barrier();
  {
    const v4u a0 = *(const LAS v4u*)(lds + a_or), a1 = *(const LAS v4u*)(lds + a_or + 64);
    const v4f f = mfma16(a1, wf[1], mfma16(a0, wf[0], v4f{0.f, 0.f, 0.f, 0.f}));
    if (l < 16) __builtin_amdgcn_raw_buffer_store_b128(v4u{U(f[0]), tag, U(f[1]), tag}, rws, 16 * l, s_fp, AUX_SC1);
  }
  STAMP(11);
  if (j < 8) {
    const int f = g * 8 + j, fc = f >> 4, col0 = 8 * (f & 15);
    const unsigned vo = (l >> 3) * 2048 + 16 * (l & 7);
    const unsigned so = WS_FP + ((fc * 64 + 8 * w) * 128 + col0) * 16;
    v4u d;
    WAITVM(0);
#pragma unroll 1
    for (unsigned spin = 0; spin < SPIN_CAP; ++spin) {
      MEMBAR();
      d = __builtin_amdgcn_raw_buffer_load_b128(rws, vo, so, AUX_SC1);
      COUNT(rounds2);
      if (__all(badtag(d, tag) == 0)) break;
    }
    STAMP(12);
    float r = fold32(F(d[0]), F(d[2]));
    r = fold16(r, r);
    r += dpp<ROR8>(r);
    *(LAS float*)(lds + L_MRG + ((l >> 5) * 8 + (l & 7)) * 32 + 4 * w) = r;
    lds_barrier();
    if (w == 0 && l < 16) {
      const v4f t = *(LAS v4f*)(lds + L_MRG + 32 * l) + *(LAS v4f*)(lds + L_MRG + 32 * l + 16);
      const float z = (t[0] + t[1]) + (t[2] + t[3]);
      ((GAS unsigned short*)ka[5])[(l >> 3) * 512 + 128 * fc + col0 + (l & 7)] = (unsigned short)pkbf(z, z);
      if (f == 0 && l == 0) __builtin_amdgcn_raw_buffer_store_b32(tag, rws, 0, WS_EPOCH, AUX_SC1);
    }
    STAMP(13);
  }
  DUMP();
}

extern "C" void kernel_launch(void* const* d_in, const int* in_sizes, int n_in, void* d_out, int out_size, void* d_ws, size_t ws_size,
                              hipStream_t stream) {
  (void)in_sizes; (void)n_in; (void)out_size; (void)ws_size;
  decode_fused<<<256, 512, 0, stream>>>(d_in[0], d_in[1], d_in[2], d_in[3], d_in[4], d_out, d_ws, 0ull);
}
```

```cpp
#define CODEPF 6656
#include <hip/hip_runtime.h>
#include <hip/hip_bf16.h>

typedef unsigned v4u __attribute__((ext_vector_type(4)));
typedef unsigned v2u __attribute__((ext_vector_type(2)));
typedef float v4f __attribute__((ext_vector_type(4)));
typedef float v2f __attribute__((ext_vector_type(2)));
typedef short v4s __attribute__((ext_vector_type(4)));
typedef __bf16 v8bf __attribute__((ext_vector_type(8)));
typedef __bf16 v2bf __attribute__((ext_vector_type(2)));
typedef unsigned long long v8q __attribute__((ext_vector_type(8)));
#define LAS __attribute__((address_space(3)))
#define GAS __attribute__((address_space(1)))
#define CAS __attribute__((address_space(4)))
#define SB() __builtin_amdgcn_sched_barrier(0)
#define MEMBAR() asm volatile("" ::: "memory")
#define WAITVM(n) do { SB(); __builtin_amdgcn_s_waitcnt((((n) & 15) | (((n) >> 4) << 14) | (7 << 4) | (15 << 8))); SB(); } while (0)

#define DEPTH 8
#define D0 4
#define GPOS 18
#define QPOS 24
#define WOAT 5
#define NOB7 0
#define CODEWG 16
#define CODEAUX 0
#define PUBW 4
constexpr int SK = 32768, WIN = 4096, DH = 128;
constexpr int AUX_NT = 2, AUX_SC1 = 16;
constexpr unsigned RSRC_FLAGS = 0x00020000u;
constexpr unsigned WS_EPOCH = 0;
constexpr unsigned WS_QP = 4096;
constexpr unsigned WS_OP = WS_QP + 8 * 2 * 4 * 2048;
constexpr unsigned WS_FP = WS_OP + 8 * 2 * 16 * 4096;
constexpr unsigned WS_END = WS_FP + 4 * 64 * 128 * 16;
constexpr unsigned WS_STAMP = 4u << 20;
constexpr int L_W = 0;
constexpr int L_MRG = 16384;
constexpr int L_Q = L_MRG + 4 * 4 * 128 * 4;
constexpr int L_MAX = L_Q + 1024;
constexpr int L_SUM = L_MAX + 64;
constexpr int L_O = L_SUM + 64;
constexpr int L_X = L_O + 256;
constexpr int L_END = L_X + 512;
constexpr unsigned SPIN_CAP = 1u << 18;

__device__ __forceinline__ v4f mfma16(v4u a, v4u b, v4f c) {
  return __builtin_amdgcn_mfma_f32_16x16x32_bf16(__builtin_bit_cast(v8bf, a), __builtin_bit_cast(v8bf, b), c, 0, 0, 0);
}
__device__ __forceinline__ unsigned pkbf(float lo, float hi) {
  v2bf r; r[0] = (__bf16)lo; r[1] = (__bf16)hi; return __builtin_bit_cast(unsigned, r);
}
template <int CTRL> __device__ __forceinline__ float dpp(float x) {
  return __builtin_bit_cast(float, __builtin_amdgcn_mov_dpp(__builtin_bit_cast(int, x), CTRL, 0xf, 0xf, true));
}
constexpr int SHL4 = 0x104, SHL8 = 0x108, SHL12 = 0x10c, SHR4 = 0x114, SHR8 = 0x118, SHR12 = 0x11c, ROR8 = 0x128;
#define F(x) __uint_as_float(x)
#define U(x) __float_as_uint(x)
__device__ __forceinline__ float vmax(float a, float b) { return __builtin_elementwise_maximum(a, b); }
__device__ __forceinline__ float fold32(float a, float b) { auto t = __builtin_amdgcn_permlane32_swap(U(a), U(b), false, false); return F(t[0]) + F(t[1]); }
__device__ __forceinline__ float fold16(float a, float b) { auto t = __builtin_amdgcn_permlane16_swap(U(a), U(b), false, false); return F(t[0]) + F(t[1]); }
__device__ __forceinline__ float foldmax32(float a, float b) { auto t = __builtin_amdgcn_permlane32_swap(U(a), U(b), false, false); return vmax(F(t[0]), F(t[1])); }
__device__ __forceinline__ float foldmax16(float a, float b) { auto t = __builtin_amdgcn_permlane16_swap(U(a), U(b), false, false); return vmax(F(t[0]), F(t[1])); }
__device__ __forceinline__ v4u tr_frag(LAS unsigned char* p, int second_off) {
  const v4s a = __builtin_amdgcn_ds_read_tr16_b64_v4i16((LAS v4s*)p);
  const v4s b = __builtin_amdgcn_ds_read_tr16_b64_v4i16((LAS v4s*)(p + second_off));
  const v2u a2 = __builtin_bit_cast(v2u, a), b2 = __builtin_bit_cast(v2u, b);
  return v4u{a2[0], a2[1], b2[0], b2[1]};
}
__device__ __forceinline__ void lds_barrier() {
  SB();
  asm volatile("s_waitcnt lgkmcnt(0)" ::: "memory");
  __builtin_amdgcn_s_barrier();
  MEMBAR();
  SB();
}
__device__ __forceinline__ unsigned badtag(v4u d, unsigned tag) { return (d[1] ^ tag) | (d[3] ^ tag); }
template <unsigned RND>
__device__ __forceinline__ v4u pack4(v4f a, unsigned tag) {
  const unsigned r0 = U(a[0]) + RND, r1 = U(a[1]) + RND, r2 = U(a[2]) + RND, r3 = U(a[3]) + RND;
  return v4u{__builtin_amdgcn_perm(r0, r1, 0x07060503u), __builtin_amdgcn_perm(r1, tag, 0x06050100u),
             __builtin_amdgcn_perm(r2, r3, 0x07060503u), __builtin_amdgcn_perm(r3, tag, 0x06050100u)};
}
__device__ __forceinline__ v4f unpack4(v4u d) {
  return v4f{F(d[0] & 0xffffff00u), F(__builtin_amdgcn_perm(d[0], d[1], 0x0403020cu)), F(d[2] & 0xffffff00u), F(__builtin_amdgcn_perm(d[2], d[3], 0x0403020cu))};
}
__device__ __forceinline__ unsigned sel(unsigned long long m, unsigned t, unsigned f) { return __builtin_amdgcn_inverse_ballot_w64(m) ? t : f; }
#define STAMP(i) do {} while (0)
#define COUNT(x) do {} while (0)
#define DSTAMP(k) do {} while (0)
#define DUMP() do {} while (0)

__global__ void __launch_bounds__(512, 2)
decode_fused(const void* x_, const void* kc_, const void* vc_, const void* wq_, const void* wo_, void* out_, void* ws_, unsigned long long pad_) {
  __shared__ __attribute__((aligned(16))) unsigned char lds_raw[L_END];
  LAS unsigned char* lds = (LAS unsigned char*)lds_raw;
  STAMP(0);
  v8q ka;
  unsigned long long pc0;
  unsigned ka_early;
  asm volatile("s_load_dword %1, %2, 0x0\n\ts_getpc_b64 %0" : "=&s"(pc0), "=&s"(ka_early) : "s"(__builtin_amdgcn_kernarg_segment_ptr()));
  SB();
  const int w = __builtin_amdgcn_readfirstlane(threadIdx.x >> 6), l = threadIdx.x & 63;
  const int bid = blockIdx.x, g = bid & 7, j = bid >> 3, b = j >> 4, s = j & 15;
  const int g4 = l >> 4, bank = (l >> 2) & 3;
  const int gn = (g + 1) & 7;
  const int rq = j >> 3, cq = j & 7;
  const int r8 = j >> 2, hh = r8 >> 1, half = r8 & 1, c = j & 3;
  __amdgpu_buffer_rsrc_t rws;
  unsigned tag;
  unsigned v_h1, s_h1, a_ow, a_or, a_tb, s_fp;
  auto tail_addresses = [&]() __attribute__((always_inline)) {
    v_h1 = (l >> 3) * 4096 + 16 * (l & 7);
    s_h1 = WS_OP + ((gn * 2 + (w >> 2)) * 16 * 4 + hh) * 1024 + (4 * half + (w & 3)) * 128;
    a_ow = L_O + (w >> 2) * 128 + (16 * (w & 3) + 4 * (l & 3) + 2 * (l >> 5) + (g4 & 1)) * 2;
    a_or = L_O + (l & 1) * 128 + 16 * g4;
    a_tb = L_W + (8 * g4 + bank) * 256 + 8 * (l & 3) + 32 * w;
    s_fp = WS_FP + ((c * 64 + gn * 8 + r8) * 128 + 16 * w) * 16;
    asm volatile("" : "+v"(v_h1), "+v"(a_ow), "+v"(a_or), "+v"(a_tb));
    asm volatile("" : "+s"(s_h1), "+s"(s_fp));
  };

  const unsigned wo_s = (gn * 512 + 64 * r8 + 16 * (w & 3)) * 1024 + 256 * c;
  auto issue_wo = [&]() __attribute__((always_inline)) {
    const __amdgpu_buffer_rsrc_t ro = __builtin_amdgcn_make_buffer_rsrc((void*)ka[4], 0, 4096 * 512 * 2, RSRC_FLAGS);
#pragma unroll
    for (int i = 0; i < 4; ++i)
      __builtin_amdgcn_raw_ptr_buffer_load_lds(ro, lds + L_W + (w & 3) * 4096 + i * 1024, 16, g4 * 1024 + 16 * (l & 15), wo_s + i * 4096, 0, AUX_NT);
    SB();
  };
  if (__builtin_expect(w < 4, 1)) {
    asm volatile("s_load_dwordx16 %0, %1, 0x0" : "=s"(ka) : "s"(__builtin_amdgcn_kernarg_segment_ptr()));
    SB();
    unsigned wq_v = (l >> 3) * 8192 + 16 * (l & 7), wq_s = (128 * rq + 32 * w) * 8192 + gn * 1024 + 128 * cq;
    unsigned x_v = (l & 4) * 256 + 16 * (l & 3), x_s = 256 * rq + 64 * w;
    unsigned koff = 256 * bank + 64 * g4 + 16 * (l & 3);
    unsigned long long kvoff = ((unsigned long long)(b * 8 + g) * SK + (SK - WIN) + s * 256) * DH * 2;
    asm volatile("" : "+v"(wq_v), "+v"(x_v), "+v"(koff));
    asm volatile("" : "+s"(wq_s), "+s"(x_s), "+s"(kvoff));
    SB();
    asm volatile("s_waitcnt lgkmcnt(0)" : "+s"(ka) : "s"(ka_early));
    DSTAMP(0);
    rws = __builtin_amdgcn_make_buffer_rsrc((void*)ka[6], 0, WS_END, RSRC_FLAGS);
    const __amdgpu_buffer_rsrc_t rq_ = __builtin_amdgcn_make_buffer_rsrc((void*)ka[3], 0, 512 * 4096 * 2, RSRC_FLAGS);
    const __amdgpu_buffer_rsrc_t rx = __builtin_amdgcn_make_buffer_rsrc((void*)ka[0], 0, 2 * 512 * 2, RSRC_FLAGS);
    v4u wv[4], kk[16], vv[16];
#pragma unroll
    for (int i = 0; i < 4; ++i) { wv[i] = __builtin_amdgcn_raw_buffer_load_b128(rq_, wq_v, wq_s + i * 65536, AUX_NT); SB(); }
    DSTAMP(1);
    const v4u xv = __builtin_amdgcn_raw_buffer_load_b128(rx, x_v, x_s, 0);
    SB();
    DSTAMP(2);
    const __amdgpu_buffer_rsrc_t rk = __builtin_amdgcn_make_buffer_rsrc((void*)(ka[1] + kvoff), 0, 65536, RSRC_FLAGS);
    const __amdgpu_buffer_rsrc_t rv = __builtin_amdgcn_make_buffer_rsrc((void*)(ka[2] + kvoff), 0, 65536, RSRC_FLAGS);
    auto stream = [&](int m) __attribute__((always_inline)) {
      if (m < 16) kk[m] = __builtin_amdgcn_raw_buffer_load_b128(rk, koff, (((m + s) & 15) * 4 + w) * 1024, AUX_NT);
      else vv[m - 16] = __builtin_amdgcn_raw_buffer_load_b128(rv, 16 * l, (((m + s) & 15) * 4 + w) * 1024, AUX_NT);
      SB();
    };
    __builtin_amdgcn_s_barrier();
    SB();
    DSTAMP(3);
#pragma unroll
    for (int m = 0; m < D0; ++m) stream(m);
    DSTAMP(4);
    tag = *(const CAS unsigned*)(ka[6] + WS_EPOCH) + 1u;
    STAMP(1);

#pragma unroll
    for (int i = 0; i < 4; ++i) { *(LAS v4u*)(lds + L_W + (4 * w + i) * 1024 + 16 * l) = wv[i]; SB(); if (i == 0) DSTAMP(5); if (i == 3) DSTAMP(6); stream(D0 + i); }
    *(LAS v4u*)(lds + L_X + (l & 4) * 64 + 64 * w + 16 * (l & 3)) = xv;
    SB();
    stream(D0 + 4);
    STAMP(2);
    lds_barrier();
    {
      LAS unsigned char* tb = lds + L_W + (8 * g4 + bank) * 128 + 8 * (l & 3) + 32 * w;
      v4u f[4], xa[4];
#pragma unroll
      for (int ks = 0; ks < 4; ++ks) { f[ks] = tr_frag(tb + ks * 32 * 128, 4 * 128); xa[ks] = *(LAS v4u*)(lds + L_X + (l & 1) * 256 + 64 * ks + 16 * g4); }
      SB();
      v4f qa = mfma16(f[0], xa[0], v4f{0.f, 0.f, 0.f, 0.f}), qb = mfma16(f[1], xa[1], v4f{0.f, 0.f, 0.f, 0.f});
      qa = mfma16(f[2], xa[2], qa); qb = mfma16(f[3], xa[3], qb);
      if ((l & 15) < 2) *(LAS v4u*)(lds + L_MRG + (l & 1) * 256 + 64 * w + 16 * g4) = pack4<0x80u>(qa + qb, tag);
      lds_barrier();
      if (PUBW == 0 && w == 0 && l < 32)
        __builtin_amdgcn_raw_buffer_store_b128(*(LAS v4u*)(lds + L_MRG + 16 * l), rws, (l >> 4) * 8192 + 16 * (l & 15), WS_QP + (gn * 8 + rq) * 2048 + 256 * cq, AUX_SC1);
      SB();
    }
    STAMP(3);
    tail_addresses();
    unsigned a_qf = L_Q + (l & 3) * 256 + 16 * (4 * g4 + bank);
    unsigned a_mw = L_MRG + (w * 4 * 128 + 8 * (l & 15) + 2 * g4) * 4, a_mr = L_MRG + (w * 128 + 16 * (l >> 3) + 4 * (l & 3)) * 4, a_ls = L_SUM + 4 * w;
    unsigned s_op = WS_OP + (((g * 2 + b) * 16 + s) * 4 + w) * 1024;
    unsigned long long m_b0 = __ballot(bank == 0), m_ml = __ballot(l & 4);
    asm volatile("" : "+v"(a_qf), "+v"(a_mw), "+v"(a_mr), "+v"(a_ls));
    asm volatile("" : "+s"(s_op), "+s"(m_b0), "+s"(m_ml));

    v4u qg[2];
    auto gather = [&]() __attribute__((always_inline)) {
      MEMBAR();
#pragma unroll
      for (int i = 0; i < 2; ++i)
        qg[i] = __builtin_amdgcn_raw_buffer_load_b128(rws, (l >> 5) * 2048 + 16 * (l & 31), WS_QP + (g * 2 + b) * 8192 + i * 4096 + w * 512, AUX_SC1);
      SB();
      COUNT(rounds0);
    };
    auto gather_bad = [&]() __attribute__((always_inline)) -> bool { return !__all(((badtag(qg[0], tag) | badtag(qg[1], tag)) & 0xffffu) == 0); };
#define STEP(m) if constexpr ((m) >= D0 + 5 && (m) < QPOS) { \
      if constexpr ((m) == GPOS) gather(); \
      WAITVM(DEPTH - 1 + (((m) >= GPOS && (m) - DEPTH < GPOS) ? 2 : 0)); \
      stream(m); }
#define STEP4(m) STEP(m) STEP(m + 1) STEP(m + 2) STEP(m + 3)
    STEP4(4) STEP4(8) STEP4(12) STEP4(16) STEP4(20) STEP4(24) STEP4(28)
#define LATE(k) if constexpr (QPOS + (k) < 32) { SB(); stream(QPOS + (k)); }
    STAMP(4);
    if (gather_bad()) {
#pragma unroll 1
      for (unsigned spin = 0; spin < SPIN_CAP; ++spin) { gather(); if (!gather_bad()) break; }
    }
    {
      const v4f a = unpack4(qg[0]) + unpack4(qg[1]);
      const float e0 = fold32(a[0], a[2]), e1 = fold32(a[1], a[3]);
      *(LAS unsigned*)(lds + L_Q + w * 256 + 8 * (l & 31) + 4 * (l >> 5)) = pkbf(e0, e1);
    }
    LATE(0)
    lds_barrier();
    STAMP(5);

    const v4u qf = *(LAS v4u*)(lds + a_qf);
    float sc[16];
    {
      v4f dq[16];
#pragma unroll
      for (int i = 0; i < 16; ++i) { dq[i] = mfma16(kk[i], qf, v4f{0.f, 0.f, 0.f, 0.f}); if (i == 7) { LATE(1) } }
      LATE(2)
#pragma unroll
      for (int i = 0; i < 16; ++i) sc[i] = dq[i][0] + dpp<SHL4>(dq[i][1]);
      LATE(3)
#pragma unroll
      for (int i = 0; i < 16; ++i) sc[i] += dpp<SHL8>(dq[i][2]);
      LATE(4)
#pragma unroll
      for (int i = 0; i < 16; ++i) { sc[i] += dpp<SHL12>(dq[i][3]); asm("" : "+v"(sc[i])); }
      LATE(5)
    }
    float mx = sc[0];
#pragma unroll
    for (int i = 1; i < 16; ++i) mx = vmax(mx, sc[i]);
    LATE(6)
    mx = foldmax16(mx, mx);
    mx = foldmax32(mx, mx);
    if (l < 4) *(LAS float*)(lds + L_MAX + (w * 4 + l) * 4) = mx;
    LATE(7) LATE(8) LATE(9) LATE(10) LATE(11) LATE(12) LATE(13) LATE(14) LATE(15)
    lds_barrier();
    DSTAMP(7);
    constexpr float CS = 0.08838834764831845f * 1.4426950408889634f;
    const LAS float* pm = (const LAS float*)(lds + L_MAX) + (l & 3);
    const float M2 = vmax(vmax(pm[0], pm[4]), vmax(pm[8], pm[12])) * CS;
#pragma unroll
    for (int i = 0; i < 16; i += 2) {
      const v2f t = v2f{sc[i], sc[i + 1]} * CS - M2;
      sc[i] = __builtin_amdgcn_exp2f(t[0]); sc[i + 1] = __builtin_amdgcn_exp2f(t[1]);
    }
    v4u pf[2][4];
#pragma unroll
    for (int u = 0; u < 2; ++u)
#pragma unroll
      for (int m = 0; m < 4; ++m) pf[u][0][m] = sel(m_b0, pkbf(sc[8 * u + 2 * m], sc[8 * u + 2 * m + 1]), 0u);
    const v4u ones = {0x3f803f80u, 0x3f803f80u, 0x3f803f80u, 0x3f803f80u};
    const v4f lsum = mfma16(pf[1][0], ones, mfma16(pf[0][0], ones, v4f{0.f, 0.f, 0.f, 0.f}));
#pragma unroll
    for (int u = 0; u < 2; ++u)
#pragma unroll
      for (int m = 0; m < 4; ++m) {
        pf[u][1][m] = U(dpp<SHR4>(F(pf[u][0][m])));
        pf[u][2][m] = U(dpp<SHR8>(F(pf[u][0][m])));
        pf[u][3][m] = U(dpp<SHR12>(F(pf[u][0][m])));
      }
    if (l == 0) *(LAS v4f*)(lds + L_SUM + 16 * w) = lsum;
    const unsigned Mw = __builtin_amdgcn_readlane(U(M2), w);
    STAMP(6);

    v4f acc[2] = {v4f{0.f, 0.f, 0.f, 0.f}, v4f{0.f, 0.f, 0.f, 0.f}};
#pragma unroll
    for (int u = 0; u < 2; ++u) {
      v4u vt[8];
#pragma unroll
      for (int m = 0; m < 4; ++m) {
#pragma unroll
        for (int d = 0; d < 8; ++d) vt[d][m] = __builtin_amdgcn_perm(vv[8 * u + 2 * m + 1][d >> 1], vv[8 * u + 2 * m][d >> 1], (d & 1) ? 0x07060302u : 0x05040100u);
        if (m < 3) SB();
      }
#pragma unroll
      for (int d = 0; d < 8; ++d) acc[d & 1] = mfma16(pf[u][d >> 1], vt[d], acc[d & 1]);
      SB();
    }
    STAMP(7);
#pragma unroll
    for (int h = 0; h < 4; ++h) *(LAS v2f*)(lds + a_mw + h * 512) = v2f{acc[0][h], acc[1][h]};
    lds_barrier();
    {
      const LAS float* pl = (const LAS float*)(lds + a_ls);
      const v4f ps = {pl[0], pl[4], pl[8], pl[12]};
      const LAS v4f* src = (const LAS v4f*)(lds + a_mr);
      const v4f o0 = src[0], o1 = src[128], o2 = src[256], o3 = src[384];
      SB();
      const v4u pk = pack4<0u>((o0 + o1) + (o2 + o3), tag);
      __builtin_amdgcn_raw_buffer_store_b128(v4u{sel(m_ml, Mw, pk[0]), pk[1], sel(m_ml, U((ps[0] + ps[1]) + (ps[2] + ps[3])), pk[2]), pk[3]}, rws, 16 * l, s_op, AUX_SC1);
    }
    STAMP(8);
    WAITVM(0);
    STAMP(14);
  } else {
    SB();
    const unsigned cpf = __builtin_amdgcn_raw_buffer_load_b32(__builtin_amdgcn_make_buffer_rsrc((void*)(pc0 & ~127ull), 0, (w == 7 && bid < CODEWG) ? CODEPF : 0, RSRC_FLAGS), 128 * l, 0, CODEAUX);
    SB();
    __builtin_amdgcn_s_barrier();
    SB();
    asm volatile("s_load_dwordx16 %0, %1, 0x0\n\ts_waitcnt lgkmcnt(0)" : "=&s"(ka) : "s"(__builtin_amdgcn_kernarg_segment_ptr()), "s"(ka_early));
    rws = __builtin_amdgcn_make_buffer_rsrc((void*)ka[6], 0, WS_END, RSRC_FLAGS);
    tag = *(const CAS unsigned*)(ka[6] + WS_EPOCH) + 1u;
    tail_addresses();
    __builtin_amdgcn_s_barrier();
    lds_barrier();
    if (PUBW == 4 && w == 4 && l < 32)
      __builtin_amdgcn_raw_buffer_store_b128(*(LAS v4u*)(lds + L_MRG + 16 * l), rws, (l >> 4) * 8192 + 16 * (l & 15), WS_QP + (gn * 8 + rq) * 2048 + 256 * cq, AUX_SC1);
    lds_barrier();
    if (WOAT == 3) issue_wo();
    __builtin_amdgcn_s_barrier();
    unsigned t0 = __builtin_amdgcn_raw_buffer_load_b32(rws, (l & 3) * 128, WS_FP + ((c * 64 + gn * 8 + r8) * 128 + 32 * (w - 4)) * 16, 0);
    unsigned t1 = __builtin_amdgcn_raw_buffer_load_b32(__builtin_amdgcn_make_buffer_rsrc((void*)ka[5], 0, (w == 4 && j < 8) ? 2048 : 0, RSRC_FLAGS), (l & 1) * 1024, (g * 8 + j) * 16, 0);
    unsigned t2 = __builtin_amdgcn_raw_buffer_load_b32(__builtin_amdgcn_make_buffer_rsrc((void*)ka[4], 0, (w == 5 && WOAT >= 5) ? 4096 * 512 * 2 : 0, RSRC_FLAGS), 0, wo_s, AUX_NT);
    SB();
    if (WOAT == 4) issue_wo();
    asm volatile("" :: "v"(t0), "v"(t1), "v"(t2));
    asm volatile("" :: "v"(cpf));
    SB();
    __builtin_amdgcn_s_barrier();
    if (WOAT == 5) issue_wo();
  }

  __builtin_amdgcn_s_barrier();
  SB();
  v4u wf[2];
  {
    v4u d[2];
    auto poll = [&]() __attribute__((always_inline)) {
      MEMBAR();
#pragma unroll
      for (int i = 0; i < 2; ++i) d[i] = __builtin_amdgcn_raw_buffer_load_b128(rws, v_h1, s_h1 + i * 32768, AUX_SC1);
      SB();
      COUNT(rounds1);
    };
    auto poll_bad = [&]() __attribute__((always_inline)) -> bool { return !__all(((badtag(d[0], tag) | badtag(d[1], tag)) & 0xffffu) == 0); };
    if (WOAT == 6 && w >= 4) issue_wo();
    poll();
    if (WOAT == 7 && w >= 4) issue_wo();
    asm volatile("s_waitcnt vmcnt(2)" ::: "memory");
    STAMP(15);
    lds_barrier();
#pragma unroll
    for (int ks = 0; ks < 2; ++ks) wf[ks] = tr_frag(lds + a_tb + ks * 32 * 256, 4 * 256);
    SB();
    STAMP(9);
    if (poll_bad()) {
#pragma unroll 1
      for (unsigned spin = 0; spin < SPIN_CAP; ++spin) { poll(); if (!poll_bad()) break; }
    }
    STAMP(10);
    const float m0 = dpp<SHL4>(F(d[0][0])), m1 = dpp<SHL4>(F(d[1][0])), l0 = dpp<SHL4>(F(d[0][2])), l1 = dpp<SHL4>(F(d[1][2]));
    float Mx = vmax(m0, m1);
    Mx = foldmax32(Mx, Mx);
    Mx = foldmax16(Mx, Mx);
    Mx = vmax(Mx, dpp<ROR8>(Mx));
    const float w0 = __builtin_amdgcn_exp2f(m0 - Mx), w1 = __builtin_amdgcn_exp2f(m1 - Mx);
    const v4f a = unpack4(d[0]) * w0 + unpack4(d[1]) * w1;
    float el = l0 * w0 + l1 * w1;
    const float t0 = fold32(a[0], a[2]), t1 = fold32(a[1], a[3]);
    el = fold32(el, el);
    float e = fold16(t0, t1);
    el = fold16(el, el);
    e += dpp<ROR8>(e);
    el += dpp<ROR8>(el);
    e *= __builtin_amdgcn_rcpf(el);
    if (!(l & 12)) *(LAS unsigned short*)(lds + a_ow) = (unsigned short)pkbf(e, e);
  }
  lds_barrier();
  {
    const v4u a0 = *(const LAS v4u*)(lds + a_or), a1 = *(const LAS v4u*)(lds + a_or + 64);
    const v4f f = mfma16(a1, wf[1], mfma16(a0, wf[0], v4f{0.f, 0.f, 0.f, 0.f}));
    if (l < 16) __builtin_amdgcn_raw_buffer_store_b128(v4u{U(f[0]), tag, U(f[1]), tag}, rws, 16 * l, s_fp, AUX_SC1);
  }
  STAMP(11);
  if (j < 8) {
    const int f = g * 8 + j, fc = f >> 4, col0 = 8 * (f & 15);
    const unsigned vo = (l >> 3) * 2048 + 16 * (l & 7);
    const unsigned so = WS_FP + ((fc * 64 + 8 * w) * 128 + col0) * 16;
    v4u d;
    WAITVM(0);
#pragma unroll 1
    for (unsigned spin = 0; spin < SPIN_CAP; ++spin) {
      MEMBAR();
      d = __builtin_amdgcn_raw_buffer_load_b128(rws, vo, so, AUX_SC1);
      COUNT(rounds2);
      if (__all(badtag(d, tag) == 0)) break;
    }
    STAMP(12);
    float r = fold32(F(d[0]), F(d[2]));
    r = fold16(r, r);
    r += dpp<ROR8>(r);
    *(LAS float*)(lds + L_MRG + ((l >> 5) * 8 + (l & 7)) * 32 + 4 * w) = r;
    lds_barrier();
    if (w == 0 && l < 16) {
      const v4f t = *(LAS v4f*)(lds + L_MRG + 32 * l) + *(LAS v4f*)(lds + L_MRG + 32 * l + 16);
      const float z = (t[0] + t[1]) + (t[2] + t[3]);
      ((GAS unsigned short*)ka[5])[(l >> 3) * 512 + 128 * fc + col0 + (l & 7)] = (unsigned short)pkbf(z, z);
      if (f == 0 && l == 0) __builtin_amdgcn_raw_buffer_store_b32(tag, rws, 0, WS_EPOCH, AUX_SC1);
    }
    STAMP(13);
  }
  DUMP();
}

extern "C" void kernel_launch(void* const* d_in, const int* in_sizes, int n_in, void* d_out, int out_size, void* d_ws, size_t ws_size,
                              hipStream_t stream) {
  (void)in_sizes; (void)n_in; (void)out_size; (void)ws_size;
  decode_fused<<<256, 512, 0, stream>>>(d_in[0], d_in[1], d_in[2], d_in[3], d_in[4], d_out, d_ws, 0ull);
}
```

```cpp
#define CODEPF 6656
#define CODETOP 1
#define CODEWG 64
#include <hip/hip_runtime.h>
#include <hip/hip_bf16.h>

typedef unsigned v4u __attribute__((ext_vector_type(4)));
typedef unsigned v2u __attribute__((ext_vector_type(2)));
typedef float v4f __attribute__((ext_vector_type(4)));
typedef float v2f __attribute__((ext_vector_type(2)));
typedef short v4s __attribute__((ext_vector_type(4)));
typedef __bf16 v8bf __attribute__((ext_vector_type(8)));
typedef __bf16 v2bf __attribute__((ext_vector_type(2)));
typedef unsigned long long v8q __attribute__((ext_vector_type(8)));
#define LAS __attribute__((address_space(3)))
#define GAS __attribute__((address_space(1)))
#define CAS __attribute__((address_space(4)))
#define SB() __builtin_amdgcn_sched_barrier(0)
#define MEMBAR() asm volatile("" ::: "memory")
#define WAITVM(n) do { SB(); __builtin_amdgcn_s_waitcnt((((n) & 15) | (((n) >> 4) << 14) | (7 << 4) | (15 << 8))); SB(); } while (0)

#define DEPTH 8
#define D0 4
#define GPOS 18
#define QPOS 24
#define WOAT 5
#define NOB7 0
#define CODEAUX 0
#define PUBW 4
constexpr int SK = 32768, WIN = 4096, DH = 128;
constexpr int AUX_NT = 2, AUX_SC1 = 16;
constexpr unsigned RSRC_FLAGS = 0x00020000u;
constexpr unsigned WS_EPOCH = 0;
constexpr unsigned WS_QP = 4096;
constexpr unsigned WS_OP = WS_QP + 8 * 2 * 4 * 2048;
constexpr unsigned WS_FP = WS_OP + 8 * 2 * 16 * 4096;
constexpr unsigned WS_END = WS_FP + 4 * 64 * 128 * 16;
constexpr unsigned WS_STAMP = 4u << 20;
constexpr int L_W = 0;
constexpr int L_MRG = 16384;
constexpr int L_Q = L_MRG + 4 * 4 * 128 * 4;
constexpr int L_MAX = L_Q + 1024;
constexpr int L_SUM = L_MAX + 64;
constexpr int L_O = L_SUM + 64;
constexpr int L_X = L_O + 256;
constexpr int L_END = L_X + 512;
constexpr unsigned SPIN_CAP = 1u << 18;

__device__ __forceinline__ v4f mfma16(v4u a, v4u b, v4f c) {
  return __builtin_amdgcn_mfma_f32_16x16x32_bf16(__builtin_bit_cast(v8bf, a), __builtin_bit_cast(v8bf, b), c, 0, 0, 0);
}
__device__ __forceinline__ unsigned pkbf(float lo, float hi) {
  v2bf r; r[0] = (__bf16)lo; r[1] = (__bf16)hi; return __builtin_bit_cast(unsigned, r);
}
template <int CTRL> __device__ __forceinline__ float dpp(float x) {
  return __builtin_bit_cast(float, __builtin_amdgcn_mov_dpp(__builtin_bit_cast(int, x), CTRL, 0xf, 0xf, true));
}
constexpr int SHL4 = 0x104, SHL8 = 0x108, SHL12 = 0x10c, SHR4 = 0x114, SHR8 = 0x118, SHR12 = 0x11c, ROR8 = 0x128;
#define F(x) __uint_as_float(x)
#define U(x) __float_as_uint(x)
__device__ __forceinline__ float vmax(float a, float b) { return __builtin_elementwise_maximum(a, b); }
__device__ __forceinline__ float fold32(float a, float b) { auto t = __builtin_amdgcn_permlane32_swap(U(a), U(b), false, false); return F(t[0]) + F(t[1]); }
__device__ __forceinline__ float fold16(float a, float b) { auto t = __builtin_amdgcn_permlane16_swap(U(a), U(b), false, false); return F(t[0]) + F(t[1]); }
__device__ __forceinline__ float foldmax32(float a, float b) { auto t = __builtin_amdgcn_permlane32_swap(U(a), U(b), false, false); return vmax(F(t[0]), F(t[1])); }
__device__ __forceinline__ float foldmax16(float a, float b) { auto t = __builtin_amdgcn_permlane16_swap(U(a), U(b), false, false); return vmax(F(t[0]), F(t[1])); }
__device__ __forceinline__ v4u tr_frag(LAS unsigned char* p, int second_off) {
  const v4s a = __builtin_amdgcn_ds_read_tr16_b64_v4i16((LAS v4s*)p);
  const v4s b = __builtin_amdgcn_ds_read_tr16_b64_v4i16((LAS v4s*)(p + second_off));
  const v2u a2 = __builtin_bit_cast(v2u, a), b2 = __builtin_bit_cast(v2u, b);
  return v4u{a2[0], a2[1], b2[0], b2[1]};
}
__device__ __forceinline__ void lds_barrier() {
  SB();
  asm volatile("s_waitcnt lgkmcnt(0)" ::: "memory");
  __builtin_amdgcn_s_barrier();
  MEMBAR();
  SB();
}
__device__ __forceinline__ unsigned badtag(v4u d, unsigned tag) { return (d[1] ^ tag) | (d[3] ^ tag); }
template <unsigned RND>
__device__ __forceinline__ v4u pack4(v4f a, unsigned tag) {
  const unsigned r0 = U(a[0]) + RND, r1 = U(a[1]) + RND, r2 = U(a[2]) + RND, r3 = U(a[3]) + RND;
  return v4u{__builtin_amdgcn_perm(r0, r1, 0x07060503u), __builtin_amdgcn_perm(r1, tag, 0x06050100u),
             __builtin_amdgcn_perm(r2, r3, 0x07060503u), __builtin_amdgcn_perm(r3, tag, 0x06050100u)};
}
__device__ __forceinline__ v4f unpack4(v4u d) {
  return v4f{F(d[0] & 0xffffff00u), F(__builtin_amdgcn_perm(d[0], d[1], 0x0403020cu)), F(d[2] & 0xffffff00u), F(__builtin_amdgcn_perm(d[2], d[3], 0x0403020cu))};
}
__device__ __forceinline__ unsigned sel(unsigned long long m, unsigned t, unsigned f) { return __builtin_amdgcn_inverse_ballot_w64(m) ? t : f; }
#define STAMP(i) do {} while (0)
#define COUNT(x) do {} while (0)
#define DSTAMP(k) do {} while (0)
#define DUMP() do {} while (0)

__global__ void __launch_bounds__(512, 2)
decode_fused(const void* x_, const void* kc_, const void* vc_, const void* wq_, const void* wo_, void* out_, void* ws_, unsigned long long pad_) {
  __shared__ __attribute__((aligned(16))) unsigned char lds_raw[L_END];
  LAS unsigned char* lds = (LAS unsigned char*)lds_raw;
  STAMP(0);
  v8q ka;
  unsigned long long pc0;
  unsigned ka_early;
  asm volatile("s_load_dword %1, %2, 0x0\n\ts_getpc_b64 %0" : "=&s"(pc0), "=&s"(ka_early) : "s"(__builtin_amdgcn_kernarg_segment_ptr()));
  SB();
  unsigned cpf;
  {
    unsigned voff = (threadIdx.x - 448u) * 128u;
    cpf = __builtin_amdgcn_raw_buffer_load_b32(__builtin_amdgcn_make_buffer_rsrc((void*)pc0, 0, blockIdx.x < CODEWG ? CODEPF : 0, RSRC_FLAGS), voff, 0, CODEAUX);
  }
  SB();
  const int w = __builtin_amdgcn_readfirstlane(threadIdx.x >> 6), l = threadIdx.x & 63;
  const int bid = blockIdx.x, g = bid & 7, j = bid >> 3, b = j >> 4, s = j & 15;
  const int g4 = l >> 4, bank = (l >> 2) & 3;
  const int gn = (g + 1) & 7;
  const int rq = j >> 3, cq = j & 7;
  const int r8 = j >> 2, hh = r8 >> 1, half = r8 & 1, c = j & 3;
  __amdgpu_buffer_rsrc_t rws;
  unsigned tag;
  unsigned v_h1, s_h1, a_ow, a_or, a_tb, s_fp;
  auto tail_addresses = [&]() __attribute__((always_inline)) {
    v_h1 = (l >> 3) * 4096 + 16 * (l & 7);
    s_h1 = WS_OP + ((gn * 2 + (w >> 2)) * 16 * 4 + hh) * 1024 + (4 * half + (w & 3)) * 128;
    a_ow = L_O + (w >> 2) * 128 + (16 * (w & 3) + 4 * (l & 3) + 2 * (l >> 5) + (g4 & 1)) * 2;
    a_or = L_O + (l & 1) * 128 + 16 * g4;
    a_tb = L_W + (8 * g4 + bank) * 256 + 8 * (l & 3) + 32 * w;
    s_fp = WS_FP + ((c * 64 + gn * 8 + r8) * 128 + 16 * w) * 16;
    asm volatile("" : "+v"(v_h1), "+v"(a_ow), "+v"(a_or), "+v"(a_tb));
    asm volatile("" : "+s"(s_h1), "+s"(s_fp));
  };

  const unsigned wo_s = (gn * 512 + 64 * r8 + 16 * (w & 3)) * 1024 + 256 * c;
  auto issue_wo = [&]() __attribute__((always_inline)) {
    const __amdgpu_buffer_rsrc_t ro = __builtin_amdgcn_make_buffer_rsrc((void*)ka[4], 0, 4096 * 512 * 2, RSRC_FLAGS);
#pragma unroll
    for (int i = 0; i < 4; ++i)
      __builtin_amdgcn_raw_ptr_buffer_load_lds(ro, lds + L_W + (w & 3) * 4096 + i * 1024, 16, g4 * 1024 + 16 * (l & 15), wo_s + i * 4096, 0, AUX_NT);
    SB();
  };
  if (__builtin_expect(w < 4, 1)) {
    asm volatile("s_load_dwordx16 %0, %1, 0x0" : "=s"(ka) : "s"(__builtin_amdgcn_kernarg_segment_ptr()));
    SB();
    unsigned wq_v = (l >> 3) * 8192 + 16 * (l & 7), wq_s = (128 * rq + 32 * w) * 8192 + gn * 1024 + 128 * cq;
    unsigned x_v = (l & 4) * 256 + 16 * (l & 3), x_s = 256 * rq + 64 * w;
    unsigned koff = 256 * bank + 64 * g4 + 16 * (l & 3);
    unsigned long long kvoff = ((unsigned long long)(b * 8 + g) * SK + (SK - WIN) + s * 256) * DH * 2;
    asm volatile("" : "+v"(wq_v), "+v"(x_v), "+v"(koff));
    asm volatile("" : "+s"(wq_s), "+s"(x_s), "+s"(kvoff));
    SB();
    asm volatile("s_waitcnt lgkmcnt(0)" : "+s"(ka) : "s"(ka_early));
    DSTAMP(0);
    rws = __builtin_amdgcn_make_buffer_rsrc((void*)ka[6], 0, WS_END, RSRC_FLAGS);
    const __amdgpu_buffer_rsrc_t rq_ = __builtin_amdgcn_make_buffer_rsrc((void*)ka[3], 0, 512 * 4096 * 2, RSRC_FLAGS);
    const __amdgpu_buffer_rsrc_t rx = __builtin_amdgcn_make_buffer_rsrc((void*)ka[0], 0, 2 * 512 * 2, RSRC_FLAGS);
    v4u wv[4], kk[16], vv[16];
#pragma unroll
    for (int i = 0; i < 4; ++i) { wv[i] = __builtin_amdgcn_raw_buffer_load_b128(rq_, wq_v, wq_s + i * 65536, AUX_NT); SB(); }
    DSTAMP(1);
    const v4u xv = __builtin_amdgcn_raw_buffer_load_b128(rx, x_v, x_s, 0);
    SB();
    DSTAMP(2);
    const __amdgpu_buffer_rsrc_t rk = __builtin_amdgcn_make_buffer_rsrc((void*)(ka[1] + kvoff), 0, 65536, RSRC_FLAGS);
    const __amdgpu_buffer_rsrc_t rv = __builtin_amdgcn_make_buffer_rsrc((void*)(ka[2] + kvoff), 0, 65536, RSRC_FLAGS);
    auto stream = [&](int m) __attribute__((always_inline)) {
      if (m < 16) kk[m] = __builtin_amdgcn_raw_buffer_load_b128(rk, koff, (((m + s) & 15) * 4 + w) * 1024, AUX_NT);
      else vv[m - 16] = __builtin_amdgcn_raw_buffer_load_b128(rv, 16 * l, (((m + s) & 15) * 4 + w) * 1024, AUX_NT);
      SB();
    };
    __builtin_amdgcn_s_barrier();
    SB();
    DSTAMP(3);
#pragma unroll
    for (int m = 0; m < D0; ++m) stream(m);
    DSTAMP(4);
    tag = *(const CAS unsigned*)(ka[6] + WS_EPOCH) + 1u;
    STAMP(1);

#pragma unroll
    for (int i = 0; i < 4; ++i) { *(LAS v4u*)(lds + L_W + (4 * w + i) * 1024 + 16 * l) = wv[i]; SB(); if (i == 0) DSTAMP(5); if (i == 3) DSTAMP(6); stream(D0 + i); }
    *(LAS v4u*)(lds + L_X + (l & 4) * 64 + 64 * w + 16 * (l & 3)) = xv;
    SB();
    stream(D0 + 4);
    STAMP(2);
    lds_barrier();
    {
      LAS unsigned char* tb = lds + L_W + (8 * g4 + bank) * 128 + 8 * (l & 3) + 32 * w;
      v4u f[4], xa[4];
#pragma unroll
      for (int ks = 0; ks < 4; ++ks) { f[ks] = tr_frag(tb + ks * 32 * 128, 4 * 128); xa[ks] = *(LAS v4u*)(lds + L_X + (l & 1) * 256 + 64 * ks + 16 * g4); }
      SB();
      v4f qa = mfma16(f[0], xa[0], v4f{0.f, 0.f, 0.f, 0.f}), qb = mfma16(f[1], xa[1], v4f{0.f, 0.f, 0.f, 0.f});
      qa = mfma16(f[2], xa[2], qa); qb = mfma16(f[3], xa[3], qb);
      if ((l & 15) < 2) *(LAS v4u*)(lds + L_MRG + (l & 1) * 256 + 64 * w + 16 * g4) = pack4<0x80u>(qa + qb, tag);
      lds_barrier();
      if (PUBW == 0 && w == 0 && l < 32)
        __builtin_amdgcn_raw_buffer_store_b128(*(LAS v4u*)(lds + L_MRG + 16 * l), rws, (l >> 4) * 8192 + 16 * (l & 15), WS_QP + (gn * 8 + rq) * 2048 + 256 * cq, AUX_SC1);
      SB();
    }
    STAMP(3);
    tail_addresses();
    unsigned a_qf = L_Q + (l & 3) * 256 + 16 * (4 * g4 + bank);
    unsigned a_mw = L_MRG + (w * 4 * 128 + 8 * (l & 15) + 2 * g4) * 4, a_mr = L_MRG + (w * 128 + 16 * (l >> 3) + 4 * (l & 3)) * 4, a_ls = L_SUM + 4 * w;
    unsigned s_op = WS_OP + (((g * 2 + b) * 16 + s) * 4 + w) * 1024;
    unsigned long long m_b0 = __ballot(bank == 0), m_ml = __ballot(l & 4);
    asm volatile("" : "+v"(a_qf), "+v"(a_mw), "+v"(a_mr), "+v"(a_ls));
    asm volatile("" : "+s"(s_op), "+s"(m_b0), "+s"(m_ml));

    v4u qg[2];
    auto gather = [&]() __attribute__((always_inline)) {
      MEMBAR();
#pragma unroll
      for (int i = 0; i < 2; ++i)
        qg[i] = __builtin_amdgcn_raw_buffer_load_b128(rws, (l >> 5) * 2048 + 16 * (l & 31), WS_QP + (g * 2 + b) * 8192 + i * 4096 + w * 512, AUX_SC1);
      SB();
      COUNT(rounds0);
    };
    auto gather_bad = [&]() __attribute__((always_inline)) -> bool { return !__all(((badtag(qg[0], tag) | badtag(qg[1], tag)) & 0xffffu) == 0); };
#define STEP(m) if constexpr ((m) >= D0 + 5 && (m) < QPOS) { \
      if constexpr ((m) == GPOS) gather(); \
      WAITVM(DEPTH - 1 + (((m) >= GPOS && (m) - DEPTH < GPOS) ? 2 : 0)); \
      stream(m); }
#define STEP4(m) STEP(m) STEP(m + 1) STEP(m + 2) STEP(m + 3)
    STEP4(4) STEP4(8) STEP4(12) STEP4(16) STEP4(20) STEP4(24) STEP4(28)
#define LATE(k) if constexpr (QPOS + (k) < 32) { SB(); stream(QPOS + (k)); }
    STAMP(4);
    if (gather_bad()) {
#pragma unroll 1
      for (unsigned spin = 0; spin < SPIN_CAP; ++spin) { gather(); if (!gather_bad()) break; }
    }
    {
      const v4f a = unpack4(qg[0]) + unpack4(qg[1]);
      const float e0 = fold32(a[0], a[2]), e1 = fold32(a[1], a[3]);
      *(LAS unsigned*)(lds + L_Q + w * 256 + 8 * (l & 31) + 4 * (l >> 5)) = pkbf(e0, e1);
    }
    LATE(0)
    lds_barrier();
    STAMP(5);

    const v4u qf = *(LAS v4u*)(lds + a_qf);
    float sc[16];
    {
      v4f dq[16];
#pragma unroll
      for (int i = 0; i < 16; ++i) { dq[i] = mfma16(kk[i], qf, v4f{0.f, 0.f, 0.f, 0.f}); if (i == 7) { LATE(1) } }
      LATE(2)
#pragma unroll
      for (int i = 0; i < 16; ++i) sc[i] = dq[i][0] + dpp<SHL4>(dq[i][1]);
      LATE(3)
#pragma unroll
      for (int i = 0; i < 16; ++i) sc[i] += dpp<SHL8>(dq[i][2]);
      LATE(4)
#pragma unroll
      for (int i = 0; i < 16; ++i) { sc[i] += dpp<SHL12>(dq[i][3]); asm("" : "+v"(sc[i])); }
      LATE(5)
    }
    float mx = sc[0];
#pragma unroll
    for (int i = 1; i < 16; ++i) mx = vmax(mx, sc[i]);
    LATE(6)
    mx = foldmax16(mx, mx);
    mx = foldmax32(mx, mx);
    if (l < 4) *(LAS float*)(lds + L_MAX + (w * 4 + l) * 4) = mx;
    LATE(7) LATE(8) LATE(9) LATE(10) LATE(11) LATE(12) LATE(13) LATE(14) LATE(15)
    lds_barrier();
    DSTAMP(7);
    constexpr float CS = 0.08838834764831845f * 1.4426950408889634f;
    const LAS float* pm = (const LAS float*)(lds + L_MAX) + (l & 3);
    const float M2 = vmax(vmax(pm[0], pm[4]), vmax(pm[8], pm[12])) * CS;
#pragma unroll
    for (int i = 0; i < 16; i += 2) {
      const v2f t = v2f{sc[i], sc[i + 1]} * CS - M2;
      sc[i] = __builtin_amdgcn_exp2f(t[0]); sc[i + 1] = __builtin_amdgcn_exp2f(t[1]);
    }
    v4u pf[2][4];
#pragma unroll
    for (int u = 0; u < 2; ++u)
#pragma unroll
      for (int m = 0; m < 4; ++m) pf[u][0][m] = sel(m_b0, pkbf(sc[8 * u + 2 * m], sc[8 * u + 2 * m + 1]), 0u);
    const v4u ones = {0x3f803f80u, 0x3f803f80u, 0x3f803f80u, 0x3f803f80u};
    const v4f lsum = mfma16(pf[1][0], ones, mfma16(pf[0][0], ones, v4f{0.f, 0.f, 0.f, 0.f}));
#pragma unroll
    for (int u = 0; u < 2; ++u)
#pragma unroll
      for (int m = 0; m < 4; ++m) {
        pf[u][1][m] = U(dpp<SHR4>(F(pf[u][0][m])));
        pf[u][2][m] = U(dpp<SHR8>(F(pf[u][0][m])));
        pf[u][3][m] = U(dpp<SHR12>(F(pf[u][0][m])));
      }
    if (l == 0) *(LAS v4f*)(lds + L_SUM + 16 * w) = lsum;
    const unsigned Mw = __builtin_amdgcn_readlane(U(M2), w);
    STAMP(6);

    v4f acc[2] = {v4f{0.f, 0.f, 0.f, 0.f}, v4f{0.f, 0.f, 0.f, 0.f}};
#pragma unroll
    for (int u = 0; u < 2; ++u) {
      v4u vt[8];
#pragma unroll
      for (int m = 0; m < 4; ++m) {
#pragma unroll
        for (int d = 0; d < 8; ++d) vt[d][m] = __builtin_amdgcn_perm(vv[8 * u + 2 * m + 1][d >> 1], vv[8 * u + 2 * m][d >> 1], (d & 1) ? 0x07060302u : 0x05040100u);
        if (m < 3) SB();
      }
#pragma unroll
      for (int d = 0; d < 8; ++d) acc[d & 1] = mfma16(pf[u][d >> 1], vt[d], acc[d & 1]);
      SB();
    }
    STAMP(7);
#pragma unroll
    for (int h = 0; h < 4; ++h) *(LAS v2f*)(lds + a_mw + h * 512) = v2f{acc[0][h], acc[1][h]};
    lds_barrier();
    {
      const LAS float* pl = (const LAS float*)(lds + a_ls);
      const v4f ps = {pl[0], pl[4], pl[8], pl[12]};
      const LAS v4f* src = (const LAS v4f*)(lds + a_mr);
      const v4f o0 = src[0], o1 = src[128], o2 = src[256], o3 = src[384];
      SB();
      const v4u pk = pack4<0u>((o0 + o1) + (o2 + o3), tag);
      __builtin_amdgcn_raw_buffer_store_b128(v4u{sel(m_ml, Mw, pk[0]), pk[1], sel(m_ml, U((ps[0] + ps[1]) + (ps[2] + ps[3])), pk[2]), pk[3]}, rws, 16 * l, s_op, AUX_SC1);
    }
    STAMP(8);
    WAITVM(0);
    STAMP(14);
  } else {
    SB();
    __builtin_amdgcn_s_barrier();
    SB();
    asm volatile("s_load_dwordx16 %0, %1, 0x0\n\ts_waitcnt lgkmcnt(0)" : "=&s"(ka) : "s"(__builtin_amdgcn_kernarg_segment_ptr()), "s"(ka_early));
    rws = __builtin_amdgcn_make_buffer_rsrc((void*)ka[6], 0, WS_END, RSRC_FLAGS);
    tag = *(const CAS unsigned*)(ka[6] + WS_EPOCH) + 1u;
    tail_addresses();
    __builtin_amdgcn_s_barrier();
    lds_barrier();
    if (PUBW == 4 && w == 4 && l < 32)
      __builtin_amdgcn_raw_buffer_store_b128(*(LAS v4u*)(lds + L_MRG + 16 * l), rws, (l >> 4) * 8192 + 16 * (l & 15), WS_QP + (gn * 8 + rq) * 2048 + 256 * cq, AUX_SC1);
    lds_barrier();
    if (WOAT == 3) issue_wo();
    __builtin_amdgcn_s_barrier();
    unsigned t0 = __builtin_amdgcn_raw_buffer_load_b32(rws, (l & 3) * 128, WS_FP + ((c * 64 + gn * 8 + r8) * 128 + 32 * (w - 4)) * 16, 0);
    unsigned t1 = __builtin_amdgcn_raw_buffer_load_b32(__builtin_amdgcn_make_buffer_rsrc((void*)ka[5], 0, (w == 4 && j < 8) ? 2048 : 0, RSRC_FLAGS), (l & 1) * 1024, (g * 8 + j) * 16, 0);
    unsigned t2 = __builtin_amdgcn_raw_buffer_load_b32(__builtin_amdgcn_make_buffer_rsrc((void*)ka[4], 0, (w == 5 && WOAT >= 5) ? 4096 * 512 * 2 : 0, RSRC_FLAGS), 0, wo_s, AUX_NT);
    SB();
    if (WOAT == 4) issue_wo();
    asm volatile("" :: "v"(t0), "v"(t1), "v"(t2));
    asm volatile("" :: "v"(cpf));
    SB();
    __builtin_amdgcn_s_barrier();
    if (WOAT == 5) issue_wo();
  }

  __builtin_amdgcn_s_barrier();
  SB();
  v4u wf[2];
  {
    v4u d[2];
    auto poll = [&]() __attribute__((always_inline)) {
      MEMBAR();
#pragma unroll
      for (int i = 0; i < 2; ++i) d[i] = __builtin_amdgcn_raw_buffer_load_b128(rws, v_h1, s_h1 + i * 32768, AUX_SC1);
      SB();
      COUNT(rounds1);
    };
    auto poll_bad = [&]() __attribute__((always_inline)) -> bool { return !__all(((badtag(d[0], tag) | badtag(d[1], tag)) & 0xffffu) == 0); };
    if (WOAT == 6 && w >= 4) issue_wo();
    poll();
    if (WOAT == 7 && w >= 4) issue_wo();
    asm volatile("s_waitcnt vmcnt(2)" ::: "memory");
    STAMP(15);
    lds_barrier();
#pragma unroll
    for (int ks = 0; ks < 2; ++ks) wf[ks] = tr_frag(lds + a_tb + ks * 32 * 256, 4 * 256);
    SB();
    STAMP(9);
    if (poll_bad()) {
#pragma unroll 1
      for (unsigned spin = 0; spin < SPIN_CAP; ++spin) { poll(); if (!poll_bad()) break; }
    }
    STAMP(10);
    const float m0 = dpp<SHL4>(F(d[0][0])), m1 = dpp<SHL4>(F(d[1][0])), l0 = dpp<SHL4>(F(d[0][2])), l1 = dpp<SHL4>(F(d[1][2]));
    float Mx = vmax(m0, m1);
    Mx = foldmax32(Mx, Mx);
    Mx = foldmax16(Mx, Mx);
    Mx = vmax(Mx, dpp<ROR8>(Mx));
    const float w0 = __builtin_amdgcn_exp2f(m0 - Mx), w1 = __builtin_amdgcn_exp2f(m1 - Mx);
    const v4f a = unpack4(d[0]) * w0 + unpack4(d[1]) * w1;
    float el = l0 * w0 + l1 * w1;
    const float t0 = fold32(a[0], a[2]), t1 = fold32(a[1], a[3]);
    el = fold32(el, el);
    float e = fold16(t0, t1);
    el = fold16(el, el);
    e += dpp<ROR8>(e);
    el += dpp<ROR8>(el);
    e *= __builtin_amdgcn_rcpf(el);
    if (!(l & 12)) *(LAS unsigned short*)(lds + a_ow) = (unsigned short)pkbf(e, e);
  }
  lds_barrier();
  {
    const v4u a0 = *(const LAS v4u*)(lds + a_or), a1 = *(const LAS v4u*)(lds + a_or + 64);
    const v4f f = mfma16(a1, wf[1], mfma16(a0, wf[0], v4f{0.f, 0.f, 0.f, 0.f}));
    if (l < 16) __builtin_amdgcn_raw_buffer_store_b128(v4u{U(f[0]), tag, U(f[1]), tag}, rws, 16 * l, s_fp, AUX_SC1);
  }
  STAMP(11);
  if (j < 8) {
    const int f = g * 8 + j, fc = f >> 4, col0 = 8 * (f & 15);
    const unsigned vo = (l >> 3) * 2048 + 16 * (l & 7);
    const unsigned so = WS_FP + ((fc * 64 + 8 * w) * 128 + col0) * 16;
    v4u d;
    WAITVM(0);
#pragma unroll 1
    for (unsigned spin = 0; spin < SPIN_CAP; ++spin) {
      MEMBAR();
      d = __builtin_amdgcn_raw_buffer_load_b128(rws, vo, so, AUX_SC1);
      COUNT(rounds2);
      if (__all(badtag(d, tag) == 0)) break;
    }
    STAMP(12);
    float r = fold32(F(d[0]), F(d[2]));
    r = fold16(r, r);
    r += dpp<ROR8>(r);
    *(LAS float*)(lds + L_MRG + ((l >> 5) * 8 + (l & 7)) * 32 + 4 * w) = r;
    lds_barrier();
    if (w == 0 && l < 16) {
      const v4f t = *(LAS v4f*)(lds + L_MRG + 32 * l) + *(LAS v4f*)(lds + L_MRG + 32 * l + 16);
      const float z = (t[0] + t[1]) + (t[2] + t[3]);
      ((GAS unsigned short*)ka[5])[(l >> 3) * 512 + 128 * fc + col0 + (l & 7)] = (unsigned short)pkbf(z, z);
      if (f == 0 && l == 0) __builtin_amdgcn_raw_buffer_store_b32(tag, rws, 0, WS_EPOCH, AUX_SC1);
    }
    STAMP(13);
  }
  DUMP();
}

extern "C" void kernel_launch(void* const* d_in, const int* in_sizes, int n_in, void* d_out, int out_size, void* d_ws, size_t ws_size,
                              hipStream_t stream) {
  (void)in_sizes; (void)n_in; (void)out_size; (void)ws_size;
  decode_fused<<<256, 512, 0, stream>>>(d_in[0], d_in[1], d_in[2], d_in[3], d_in[4], d_out, d_ws, 0ull);
}
```

```cpp
#define PROBE 0
#define PROBED 3
#define CODEPF 6656
#include <hip/hip_runtime.h>
#include <hip/hip_bf16.h>

typedef unsigned v4u __attribute__((ext_vector_type(4)));
typedef unsigned v2u __attribute__((ext_vector_type(2)));
typedef float v4f __attribute__((ext_vector_type(4)));
typedef float v2f __attribute__((ext_vector_type(2)));
typedef short v4s __attribute__((ext_vector_type(4)));
typedef __bf16 v8bf __attribute__((ext_vector_type(8)));
typedef __bf16 v2bf __attribute__((ext_vector_type(2)));
typedef unsigned long long v8q __attribute__((ext_vector_type(8)));
#define LAS __attribute__((address_space(3)))
#define GAS __attribute__((address_space(1)))
#define CAS __attribute__((address_space(4)))
#define SB() __builtin_amdgcn_sched_barrier(0)
#define MEMBAR() asm volatile("" ::: "memory")
#define WAITVM(n) do { SB(); __builtin_amdgcn_s_waitcnt((((n) & 15) | (((n) >> 4) << 14) | (7 << 4) | (15 << 8))); SB(); } while (0)

#define DEPTH 8
#define D0 4
#define GPOS 18
#define QPOS 24
#define WOAT 5
#define NOB7 0
#define CODEWG 16
#define CODEAUX 0
#define CODETOP 0
#define PUBW 4
constexpr int SK = 32768, WIN = 4096, DH = 128;
constexpr int AUX_NT = 2, AUX_SC1 = 16;
constexpr unsigned RSRC_FLAGS = 0x00020000u;
constexpr unsigned WS_EPOCH = 0;
constexpr unsigned WS_QP = 4096;
constexpr unsigned WS_OP = WS_QP + 8 * 2 * 4 * 2048;
constexpr unsigned WS_FP = WS_OP + 8 * 2 * 16 * 4096;
constexpr unsigned WS_END = WS_FP + 4 * 64 * 128 * 16;
constexpr unsigned WS_STAMP = 4u << 20;
constexpr int L_W = 0;
constexpr int L_MRG = 16384;
constexpr int L_Q = L_MRG + 4 * 4 * 128 * 4;
constexpr int L_MAX = L_Q + 1024;
constexpr int L_SUM = L_MAX + 64;
constexpr int L_O = L_SUM + 64;
constexpr int L_X = L_O + 256;
constexpr int L_END = L_X + 512;
constexpr unsigned SPIN_CAP = 1u << 18;

__device__ __forceinline__ v4f mfma16(v4u a, v4u b, v4f c) {
  return __builtin_amdgcn_mfma_f32_16x16x32_bf16(__builtin_bit_cast(v8bf, a), __builtin_bit_cast(v8bf, b), c, 0, 0, 0);
}
__device__ __forceinline__ unsigned pkbf(float lo, float hi) {
  v2bf r; r[0] = (__bf16)lo; r[1] = (__bf16)hi; return __builtin_bit_cast(unsigned, r);
}
template <int CTRL> __device__ __forceinline__ float dpp(float x) {
  return __builtin_bit_cast(float, __builtin_amdgcn_mov_dpp(__builtin_bit_cast(int, x), CTRL, 0xf, 0xf, true));
}
constexpr int SHL4 = 0x104, SHL8 = 0x108, SHL12 = 0x10c, SHR4 = 0x114, SHR8 = 0x118, SHR12 = 0x11c, ROR8 = 0x128;
#define F(x) __uint_as_float(x)
#define U(x) __float_as_uint(x)
__device__ __forceinline__ float vmax(float a, float b) { return __builtin_elementwise_maximum(a, b); }
__device__ __forceinline__ float fold32(float a, float b) { auto t = __builtin_amdgcn_permlane32_swap(U(a), U(b), false, false); return F(t[0]) + F(t[1]); }
__device__ __forceinline__ float fold16(float a, float b) { auto t = __builtin_amdgcn_permlane16_swap(U(a), U(b), false, false); return F(t[0]) + F(t[1]); }
__device__ __forceinline__ float foldmax32(float a, float b) { auto t = __builtin_amdgcn_permlane32_swap(U(a), U(b), false, false); return vmax(F(t[0]), F(t[1])); }
__device__ __forceinline__ float foldmax16(float a, float b) { auto t = __builtin_amdgcn_permlane16_swap(U(a), U(b), false, false); return vmax(F(t[0]), F(t[1])); }
__device__ __forceinline__ v4u tr_frag(LAS unsigned char* p, int second_off) {
  const v4s a = __builtin_amdgcn_ds_read_tr16_b64_v4i16((LAS v4s*)p);
  const v4s b = __builtin_amdgcn_ds_read_tr16_b64_v4i16((LAS v4s*)(p + second_off));
  const v2u a2 = __builtin_bit_cast(v2u, a), b2 = __builtin_bit_cast(v2u, b);
  return v4u{a2[0], a2[1], b2[0], b2[1]};
}
__device__ __forceinline__ void lds_barrier() {
  SB();
  asm volatile("s_waitcnt lgkmcnt(0)" ::: "memory");
  __builtin_amdgcn_s_barrier();
  MEMBAR();
  SB();
}
__device__ __forceinline__ unsigned badtag(v4u d, unsigned tag) { return (d[1] ^ tag) | (d[3] ^ tag); }
template <unsigned RND>
__device__ __forceinline__ v4u pack4(v4f a, unsigned tag) {
  const unsigned r0 = U(a[0]) + RND, r1 = U(a[1]) + RND, r2 = U(a[2]) + RND, r3 = U(a[3]) + RND;
  return v4u{__builtin_amdgcn_perm(r0, r1, 0x07060503u), __builtin_amdgcn_perm(r1, tag, 0x06050100u),
             __builtin_amdgcn_perm(r2, r3, 0x07060503u), __builtin_amdgcn_perm(r3, tag, 0x06050100u)};
}
__device__ __forceinline__ v4f unpack4(v4u d) {
  return v4f{F(d[0] & 0xffffff00u), F(__builtin_amdgcn_perm(d[0], d[1], 0x0403020cu)), F(d[2] & 0xffffff00u), F(__builtin_amdgcn_perm(d[2], d[3], 0x0403020cu))};
}
__device__ __forceinline__ unsigned sel(unsigned long long m, unsigned t, unsigned f) { return __builtin_amdgcn_inverse_ballot_w64(m) ? t : f; }
#define STAMP(i) do { SB(); ts[i] = __builtin_amdgcn_s_memrealtime(); SB(); } while (0)
#define COUNT(x) ++x
#define DSTAMP(k) do { SB(); td[k] = (unsigned)__builtin_amdgcn_s_memrealtime(); SB(); } while (0)
#define DUMP() do { if (td[PROBED]) { const unsigned long long e = __builtin_amdgcn_s_memrealtime() + 16 * (td[PROBED] - (unsigned)ts[0]) + (ts[0] >> 62); while (__builtin_amdgcn_s_memrealtime() < e) __builtin_amdgcn_s_sleep(2); } } while (0)

__global__ void __launch_bounds__(512, 2)
decode_fused(const void* x_, const void* kc_, const void* vc_, const void* wq_, const void* wo_, void* out_, void* ws_, unsigned long long pad_) {
  __shared__ __attribute__((aligned(16))) unsigned char lds_raw[L_END];
  LAS unsigned char* lds = (LAS unsigned char*)lds_raw;
  unsigned long long ts[16] = {0, 0, 0, 0, 0, 0, 0, 0, 0, 0, 0, 0, 0, 0, 0, 0};
  unsigned rounds0 = 0, rounds1 = 0, rounds2 = 0;
  unsigned td[8] = {0, 0, 0, 0, 0, 0, 0, 0};
  STAMP(0);
  v8q ka;
  unsigned long long pc0;
  unsigned ka_early;
  asm volatile("s_load_dword %1, %2, 0x0\n\ts_getpc_b64 %0" : "=&s"(pc0), "=&s"(ka_early) : "s"(__builtin_amdgcn_kernarg_segment_ptr()));
  SB();
  const int w = __builtin_amdgcn_readfirstlane(threadIdx.x >> 6), l = threadIdx.x & 63;
  const int bid = blockIdx.x, g = bid & 7, j = bid >> 3, b = j >> 4, s = j & 15;
  const int g4 = l >> 4, bank = (l >> 2) & 3;
  const int gn = (g + 1) & 7;
  const int rq = j >> 3, cq = j & 7;
  const int r8 = j >> 2, hh = r8 >> 1, half = r8 & 1, c = j & 3;
  __amdgpu_buffer_rsrc_t rws;
  unsigned tag;
  unsigned v_h1, s_h1, a_ow, a_or, a_tb, s_fp;
  auto tail_addresses = [&]() __attribute__((always_inline)) {
    v_h1 = (l >> 3) * 4096 + 16 * (l & 7);
    s_h1 = WS_OP + ((gn * 2 + (w >> 2)) * 16 * 4 + hh) * 1024 + (4 * half + (w & 3)) * 128;
    a_ow = L_O + (w >> 2) * 128 + (16 * (w & 3) + 4 * (l & 3) + 2 * (l >> 5) + (g4 & 1)) * 2;
    a_or = L_O + (l & 1) * 128 + 16 * g4;
    a_tb = L_W + (8 * g4 + bank) * 256 + 8 * (l & 3) + 32 * w;
    s_fp = WS_FP + ((c * 64 + gn * 8 + r8) * 128 + 16 * w) * 16;
    asm volatile("" : "+v"(v_h1), "+v"(a_ow), "+v"(a_or), "+v"(a_tb));
    asm volatile("" : "+s"(s_h1), "+s"(s_fp));
  };

  const unsigned wo_s = (gn * 512 + 64 * r8 + 16 * (w & 3)) * 1024 + 256 * c;
  auto issue_wo = [&]() __attribute__((always_inline)) {
    const __amdgpu_buffer_rsrc_t ro = __builtin_amdgcn_make_buffer_rsrc((void*)ka[4], 0, 4096 * 512 * 2, RSRC_FLAGS);
#pragma unroll
    for (int i = 0; i < 4; ++i)
      __builtin_amdgcn_raw_ptr_buffer_load_lds(ro, lds + L_W + (w & 3) * 4096 + i * 1024, 16, g4 * 1024 + 16 * (l & 15), wo_s + i * 4096, 0, AUX_NT);
    SB();
  };
  if (__builtin_expect(w < 4, 1)) {
    asm volatile("s_load_dwordx16 %0, %1, 0x0" : "=s"(ka) : "s"(__builtin_amdgcn_kernarg_segment_ptr()));
    SB();
    unsigned wq_v = (l >> 3) * 8192 + 16 * (l & 7), wq_s = (128 * rq + 32 * w) * 8192 + gn * 1024 + 128 * cq;
    unsigned x_v = (l & 4) * 256 + 16 * (l & 3), x_s = 256 * rq + 64 * w;
    unsigned koff = 256 * bank + 64 * g4 + 16 * (l & 3);
    unsigned long long kvoff = ((unsigned long long)(b * 8 + g) * SK + (SK - WIN) + s * 256) * DH * 2;
    asm volatile("" : "+v"(wq_v), "+v"(x_v), "+v"(koff));
    asm volatile("" : "+s"(wq_s), "+s"(x_s), "+s"(kvoff));
    SB();
    { SB(); unsigned long long tt; asm volatile("s_memrealtime %0" : "=s"(tt)); SB(); asm volatile("s_waitcnt lgkmcnt(0)" : "+s"(ka), "+s"(tt) : "s"(ka_early)); td[3] = (unsigned)tt; }
    DSTAMP(0);
    rws = __builtin_amdgcn_make_buffer_rsrc((void*)ka[6], 0, WS_END, RSRC_FLAGS);
    const __amdgpu_buffer_rsrc_t rq_ = __builtin_amdgcn_make_buffer_rsrc((void*)ka[3], 0, 512 * 4096 * 2, RSRC_FLAGS);
    const __amdgpu_buffer_rsrc_t rx = __builtin_amdgcn_make_buffer_rsrc((void*)ka[0], 0, 2 * 512 * 2, RSRC_FLAGS);
    v4u wv[4], kk[16], vv[16];
#pragma unroll
    for (int i = 0; i < 4; ++i) { wv[i] = __builtin_amdgcn_raw_buffer_load_b128(rq_, wq_v, wq_s + i * 65536, AUX_NT); SB(); }
    DSTAMP(1);
    const v4u xv = __builtin_amdgcn_raw_buffer_load_b128(rx, x_v, x_s, 0);
    SB();
    DSTAMP(2);
    const __amdgpu_buffer_rsrc_t rk = __builtin_amdgcn_make_buffer_rsrc((void*)(ka[1] + kvoff), 0, 65536, RSRC_FLAGS);
    const __amdgpu_buffer_rsrc_t rv = __builtin_amdgcn_make_buffer_rsrc((void*)(ka[2] + kvoff), 0, 65536, RSRC_FLAGS);
    auto stream = [&](int m) __attribute__((always_inline)) {
      if (m < 16) kk[m] = __builtin_amdgcn_raw_buffer_load_b128(rk, koff, (((m + s) & 15) * 4 + w) * 1024, AUX_NT);
      else vv[m - 16] = __builtin_amdgcn_raw_buffer_load_b128(rv, 16 * l, (((m + s) & 15) * 4 + w) * 1024, AUX_NT);
      SB();
    };
    __builtin_amdgcn_s_barrier();
    SB();
#pragma unroll
    for (int m = 0; m < D0; ++m) stream(m);
    DSTAMP(4);
    tag = *(const CAS unsigned*)(ka[6] + WS_EPOCH) + 1u;
    STAMP(1);

#pragma unroll
    for (int i = 0; i < 4; ++i) { *(LAS v4u*)(lds + L_W + (4 * w + i) * 1024 + 16 * l) = wv[i]; SB(); if (i == 0) DSTAMP(5); if (i == 3) DSTAMP(6); stream(D0 + i); }
    *(LAS v4u*)(lds + L_X + (l & 4) * 64 + 64 * w + 16 * (l & 3)) = xv;
    SB();
    stream(D0 + 4);
    STAMP(2);
    lds_barrier();
    {
      LAS unsigned char* tb = lds + L_W + (8 * g4 + bank) * 128 + 8 * (l & 3) + 32 * w;
      v4u f[4], xa[4];
#pragma unroll
      for (int ks = 0; ks < 4; ++ks) { f[ks] = tr_frag(tb + ks * 32 * 128, 4 * 128); xa[ks] = *(LAS v4u*)(lds + L_X + (l & 1) * 256 + 64 * ks + 16 * g4); }
      SB();
      v4f qa = mfma16(f[0], xa[0], v4f{0.f, 0.f, 0.f, 0.f}), qb = mfma16(f[1], xa[1], v4f{0.f, 0.f, 0.f, 0.f});
      qa = mfma16(f[2], xa[2], qa); qb = mfma16(f[3], xa[3], qb);
      if ((l & 15) < 2) *(LAS v4u*)(lds + L_MRG + (l & 1) * 256 + 64 * w + 16 * g4) = pack4<0x80u>(qa + qb, tag);
      lds_barrier();
      if (PUBW == 0 && w == 0 && l < 32)
        __builtin_amdgcn_raw_buffer_store_b128(*(LAS v4u*)(lds + L_MRG + 16 * l), rws, (l >> 4) * 8192 + 16 * (l & 15), WS_QP + (gn * 8 + rq) * 2048 + 256 * cq, AUX_SC1);
      SB();
    }
    STAMP(3);
    tail_addresses();
    unsigned a_qf = L_Q + (l & 3) * 256 + 16 * (4 * g4 + bank);
    unsigned a_mw = L_MRG + (w * 4 * 128 + 8 * (l & 15) + 2 * g4) * 4, a_mr = L_MRG + (w * 128 + 16 * (l >> 3) + 4 * (l & 3)) * 4, a_ls = L_SUM + 4 * w;
    unsigned s_op = WS_OP + (((g * 2 + b) * 16 + s) * 4 + w) * 1024;
    unsigned long long m_b0 = __ballot(bank == 0), m_ml = __ballot(l & 4);
    asm volatile("" : "+v"(a_qf), "+v"(a_mw), "+v"(a_mr), "+v"(a_ls));
    asm volatile("" : "+s"(s_op), "+s"(m_b0), "+s"(m_ml));

    v4u qg[2];
    auto gather = [&]() __attribute__((always_inline)) {
      MEMBAR();
#pragma unroll
      for (int i = 0; i < 2; ++i)
        qg[i] = __builtin_amdgcn_raw_buffer_load_b128(rws, (l >> 5) * 2048 + 16 * (l & 31), WS_QP + (g * 2 + b) * 8192 + i * 4096 + w * 512, AUX_SC1);
      SB();
      COUNT(rounds0);
    };
    auto gather_bad = [&]() __attribute__((always_inline)) -> bool { return !__all(((badtag(qg[0], tag) | badtag(qg[1], tag)) & 0xffffu) == 0); };
#define STEP(m) if constexpr ((m) >= D0 + 5 && (m) < QPOS) { \
      if constexpr ((m) == GPOS) gather(); \
      WAITVM(DEPTH - 1 + (((m) >= GPOS && (m) - DEPTH < GPOS) ? 2 : 0)); \
      stream(m); }
#define STEP4(m) STEP(m) STEP(m + 1) STEP(m + 2) STEP(m + 3)
    STEP4(4) STEP4(8) STEP4(12) STEP4(16) STEP4(20) STEP4(24) STEP4(28)
#define LATE(k) if constexpr (QPOS + (k) < 32) { SB(); stream(QPOS + (k)); }
    STAMP(4);
    if (gather_bad()) {
#pragma unroll 1
      for (unsigned spin = 0; spin < SPIN_CAP; ++spin) { gather(); if (!gather_bad()) break; }
    }
    {
      const v4f a = unpack4(qg[0]) + unpack4(qg[1]);
      const float e0 = fold32(a[0], a[2]), e1 = fold32(a[1], a[3]);
      *(LAS unsigned*)(lds + L_Q + w * 256 + 8 * (l & 31) + 4 * (l >> 5)) = pkbf(e0, e1);
    }
    LATE(0)
    lds_barrier();
    STAMP(5);

    const v4u qf = *(LAS v4u*)(lds + a_qf);
    float sc[16];
    {
      v4f dq[16];
#pragma unroll
      for (int i = 0; i < 16; ++i) { dq[i] = mfma16(kk[i], qf, v4f{0.f, 0.f, 0.f, 0.f}); if (i == 7) { LATE(1) } }
      LATE(2)
#pragma unroll
      for (int i = 0; i < 16; ++i) sc[i] = dq[i][0] + dpp<SHL4>(dq[i][1]);
      LATE(3)
#pragma unroll
      for (int i = 0; i < 16; ++i) sc[i] += dpp<SHL8>(dq[i][2]);
      LATE(4)
#pragma unroll
      for (int i = 0; i < 16; ++i) { sc[i] += dpp<SHL12>(dq[i][3]); asm("" : "+v"(sc[i])); }
      LATE(5)
    }
    float mx = sc[0];
#pragma unroll
    for (int i = 1; i < 16; ++i) mx = vmax(mx, sc[i]);
    LATE(6)
    mx = foldmax16(mx, mx);
    mx = foldmax32(mx, mx);
    if (l < 4) *(LAS float*)(lds + L_MAX + (w * 4 + l) * 4) = mx;
    LATE(7) LATE(8) LATE(9) LATE(10) LATE(11) LATE(12) LATE(13) LATE(14) LATE(15)
    lds_barrier();
    DSTAMP(7);
    constexpr float CS = 0.08838834764831845f * 1.4426950408889634f;
    const LAS float* pm = (const LAS float*)(lds + L_MAX) + (l & 3);
    const float M2 = vmax(vmax(pm[0], pm[4]), vmax(pm[8], pm[12])) * CS;
#pragma unroll
    for (int i = 0; i < 16; i += 2) {
      const v2f t = v2f{sc[i], sc[i + 1]} * CS - M2;
      sc[i] = __builtin_amdgcn_exp2f(t[0]); sc[i + 1] = __builtin_amdgcn_exp2f(t[1]);
    }
    v4u pf[2][4];
#pragma unroll
    for (int u = 0; u < 2; ++u)
#pragma unroll
      for (int m = 0; m < 4; ++m) pf[u][0][m] = sel(m_b0, pkbf(sc[8 * u + 2 * m], sc[8 * u + 2 * m + 1]), 0u);
    const v4u ones = {0x3f803f80u, 0x3f803f80u, 0x3f803f80u, 0x3f803f80u};
    const v4f lsum = mfma16(pf[1][0], ones, mfma16(pf[0][0], ones, v4f{0.f, 0.f, 0.f, 0.f}));
#pragma unroll
    for (int u = 0; u < 2; ++u)
#pragma unroll
      for (int m = 0; m < 4; ++m) {
        pf[u][1][m] = U(dpp<SHR4>(F(pf[u][0][m])));
        pf[u][2][m] = U(dpp<SHR8>(F(pf[u][0][m])));
        pf[u][3][m] = U(dpp<SHR12>(F(pf[u][0][m])));
      }
    if (l == 0) *(LAS v4f*)(lds + L_SUM + 16 * w) = lsum;
    const unsigned Mw = __builtin_amdgcn_readlane(U(M2), w);
    STAMP(6);

    v4f acc[2] = {v4f{0.f, 0.f, 0.f, 0.f}, v4f{0.f, 0.f, 0.f, 0.f}};
#pragma unroll
    for (int u = 0; u < 2; ++u) {
      v4u vt[8];
#pragma unroll
      for (int m = 0; m < 4; ++m) {
#pragma unroll
        for (int d = 0; d < 8; ++d) vt[d][m] = __builtin_amdgcn_perm(vv[8 * u + 2 * m + 1][d >> 1], vv[8 * u + 2 * m][d >> 1], (d & 1) ? 0x07060302u : 0x05040100u);
        if (m < 3) SB();
      }
#pragma unroll
      for (int d = 0; d < 8; ++d) acc[d & 1] = mfma16(pf[u][d >> 1], vt[d], acc[d & 1]);
      SB();
    }
    STAMP(7);
#pragma unroll
    for (int h = 0; h < 4; ++h) *(LAS v2f*)(lds + a_mw + h * 512) = v2f{acc[0][h], acc[1][h]};
    lds_barrier();
    {
      const LAS float* pl = (const LAS float*)(lds + a_ls);
      const v4f ps = {pl[0], pl[4], pl[8], pl[12]};
      const LAS v4f* src = (const LAS v4f*)(lds + a_mr);
      const v4f o0 = src[0], o1 = src[128], o2 = src[256], o3 = src[384];
      SB();
      const v4u pk = pack4<0u>((o0 + o1) + (o2 + o3), tag);
      __builtin_amdgcn_raw_buffer_store_b128(v4u{sel(m_ml, Mw, pk[0]), pk[1], sel(m_ml, U((ps[0] + ps[1]) + (ps[2] + ps[3])), pk[2]), pk[3]}, rws, 16 * l, s_op, AUX_SC1);
    }
    STAMP(8);
    WAITVM(0);
    STAMP(14);
  } else {
    SB();
    const unsigned cpf = __builtin_amdgcn_raw_buffer_load_b32(__builtin_amdgcn_make_buffer_rsrc((void*)(pc0 & ~127ull), 0, (w == 7 && bid < CODEWG) ? CODEPF : 0, RSRC_FLAGS), 128 * l, 0, CODEAUX);
    SB();
    __builtin_amdgcn_s_barrier();
    SB();
    asm volatile("s_load_dwordx16 %0, %1, 0x0\n\ts_waitcnt lgkmcnt(0)" : "=&s"(ka) : "s"(__builtin_amdgcn_kernarg_segment_ptr()), "s"(ka_early));
    rws = __builtin_amdgcn_make_buffer_rsrc((void*)ka[6], 0, WS_END, RSRC_FLAGS);
    tag = *(const CAS unsigned*)(ka[6] + WS_EPOCH) + 1u;
    tail_addresses();
    __builtin_amdgcn_s_barrier();
    lds_barrier();
    if (PUBW == 4 && w == 4 && l < 32)
      __builtin_amdgcn_raw_buffer_store_b128(*(LAS v4u*)(lds + L_MRG + 16 * l), rws, (l >> 4) * 8192 + 16 * (l & 15), WS_QP + (gn * 8 + rq) * 2048 + 256 * cq, AUX_SC1);
    lds_barrier();
    if (WOAT == 3) issue_wo();
    __builtin_amdgcn_s_barrier();
    unsigned t0 = __builtin_amdgcn_raw_buffer_load_b32(rws, (l & 3) * 128, WS_FP + ((c * 64 + gn * 8 + r8) * 128 + 32 * (w - 4)) * 16, 0);
    unsigned t1 = __builtin_amdgcn_raw_buffer_load_b32(__builtin_amdgcn_make_buffer_rsrc((void*)ka[5], 0, (w == 4 && j < 8) ? 2048 : 0, RSRC_FLAGS), (l & 1) * 1024, (g * 8 + j) * 16, 0);
    unsigned t2 = __builtin_amdgcn_raw_buffer_load_b32(__builtin_amdgcn_make_buffer_rsrc((void*)ka[4], 0, (w == 5 && WOAT >= 5) ? 4096 * 512 * 2 : 0, RSRC_FLAGS), 0, wo_s, AUX_NT);
    SB();
    if (WOAT == 4) issue_wo();
    asm volatile("" :: "v"(t0), "v"(t1), "v"(t2));
    asm volatile("" :: "v"(cpf));
    SB();
    __builtin_amdgcn_s_barrier();
    if (WOAT == 5) issue_wo();
  }

  __builtin_amdgcn_s_barrier();
  SB();
  v4u wf[2];
  {
    v4u d[2];
    auto poll = [&]() __attribute__((always_inline)) {
      MEMBAR();
#pragma unroll
      for (int i = 0; i < 2; ++i) d[i] = __builtin_amdgcn_raw_buffer_load_b128(rws, v_h1, s_h1 + i * 32768, AUX_SC1);
      SB();
      COUNT(rounds1);
    };
    auto poll_bad = [&]() __attribute__((always_inline)) -> bool { return !__all(((badtag(d[0], tag) | badtag(d[1], tag)) & 0xffffu) == 0); };
    if (WOAT == 6 && w >= 4) issue_wo();
    poll();
    if (WOAT == 7 && w >= 4) issue_wo();
    asm volatile("s_waitcnt vmcnt(2)" ::: "memory");
    STAMP(15);
    lds_barrier();
#pragma unroll
    for (int ks = 0; ks < 2; ++ks) wf[ks] = tr_frag(lds + a_tb + ks * 32 * 256, 4 * 256);
    SB();
    STAMP(9);
    if (poll_bad()) {
#pragma unroll 1
      for (unsigned spin = 0; spin < SPIN_CAP; ++spin) { poll(); if (!poll_bad()) break; }
    }
    STAMP(10);
    const float m0 = dpp<SHL4>(F(d[0][0])), m1 = dpp<SHL4>(F(d[1][0])), l0 = dpp<SHL4>(F(d[0][2])), l1 = dpp<SHL4>(F(d[1][2]));
    float Mx = vmax(m0, m1);
    Mx = foldmax32(Mx, Mx);
    Mx = foldmax16(Mx, Mx);
    Mx = vmax(Mx, dpp<ROR8>(Mx));
    const float w0 = __builtin_amdgcn_exp2f(m0 - Mx), w1 = __builtin_amdgcn_exp2f(m1 - Mx);
    const v4f a = unpack4(d[0]) * w0 + unpack4(d[1]) * w1;
    float el = l0 * w0 + l1 * w1;
    const float t0 = fold32(a[0], a[2]), t1 = fold32(a[1], a[3]);
    el = fold32(el, el);
    float e = fold16(t0, t1);
    el = fold16(el, el);
    e += dpp<ROR8>(e);
    el += dpp<ROR8>(el);
    e *= __builtin_amdgcn_rcpf(el);
    if (!(l & 12)) *(LAS unsigned short*)(lds + a_ow) = (unsigned short)pkbf(e, e);
  }
  lds_barrier();
  {
    const v4u a0 = *(const LAS v4u*)(lds + a_or), a1 = *(const LAS v4u*)(lds + a_or + 64);
    const v4f f = mfma16(a1, wf[1], mfma16(a0, wf[0], v4f{0.f, 0.f, 0.f, 0.f}));
    if (l < 16) __builtin_amdgcn_raw_buffer_store_b128(v4u{U(f[0]), tag, U(f[1]), tag}, rws, 16 * l, s_fp, AUX_SC1);
  }
  STAMP(11);
  if (j < 8) {
    const int f = g * 8 + j, fc = f >> 4, col0 = 8 * (f & 15);
    const unsigned vo = (l >> 3) * 2048 + 16 * (l & 7);
    const unsigned so = WS_FP + ((fc * 64 + 8 * w) * 128 + col0) * 16;
    v4u d;
    WAITVM(0);
#pragma unroll 1
    for (unsigned spin = 0; spin < SPIN_CAP; ++spin) {
      MEMBAR();
      d = __builtin_amdgcn_raw_buffer_load_b128(rws, vo, so, AUX_SC1);
      COUNT(rounds2);
      if (__all(badtag(d, tag) == 0)) break;
    }
    STAMP(12);
    float r = fold32(F(d[0]), F(d[2]));
    r = fold16(r, r);
    r += dpp<ROR8>(r);
    *(LAS float*)(lds + L_MRG + ((l >> 5) * 8 + (l & 7)) * 32 + 4 * w) = r;
    lds_barrier();
    if (w == 0 && l < 16) {
      const v4f t = *(LAS v4f*)(lds + L_MRG + 32 * l) + *(LAS v4f*)(lds + L_MRG + 32 * l + 16);
      const float z = (t[0] + t[1]) + (t[2] + t[3]);
      ((GAS unsigned short*)ka[5])[(l >> 3) * 512 + 128 * fc + col0 + (l & 7)] = (unsigned short)pkbf(z, z);
      if (f == 0 && l == 0) __builtin_amdgcn_raw_buffer_store_b32(tag, rws, 0, WS_EPOCH, AUX_SC1);
    }
    STAMP(13);
  }
  DUMP();
}

extern "C" void kernel_launch(void* const* d_in, const int* in_sizes, int n_in, void* d_out, int out_size, void* d_ws, size_t ws_size,
                              hipStream_t stream) {
  (void)in_sizes; (void)n_in; (void)out_size; (void)ws_size;
  decode_fused<<<256, 512, 0, stream>>>(d_in[0], d_in[1], d_in[2], d_in[3], d_in[4], d_out, d_ws, 0ull);
}
```

```cpp
#include <hip/hip_runtime.h>
#include <hip/hip_bf16.h>

typedef unsigned v4u __attribute__((ext_vector_type(4)));
typedef unsigned v2u __attribute__((ext_vector_type(2)));
typedef float v4f __attribute__((ext_vector_type(4)));
typedef float v2f __attribute__((ext_vector_type(2)));
typedef short v4s __attribute__((ext_vector_type(4)));
typedef __bf16 v8bf __attribute__((ext_vector_type(8)));
typedef __bf16 v2bf __attribute__((ext_vector_type(2)));
typedef unsigned long long v8q __attribute__((ext_vector_type(8)));
#define LAS __attribute__((address_space(3)))
#define GAS __attribute__((address_space(1)))
#define CAS __attribute__((address_space(4)))
#define SB() __builtin_amdgcn_sched_barrier(0)
#define MEMBAR() asm volatile("" ::: "memory")
#define WAITVM(n) do { SB(); __builtin_amdgcn_s_waitcnt((((n) & 15) | (((n) >> 4) << 14) | (7 << 4) | (15 << 8))); SB(); } while (0)
#define PIN8(u) asm("" : "+v"(u[0]), "+v"(u[1]), "+v"(u[2]), "+v"(u[3]), "+v"(u[4]), "+v"(u[5]), "+v"(u[6]), "+v"(u[7]))
#define F(x) __uint_as_float(x)
#define U(x) __float_as_uint(x)

constexpr int SK = 32768, WIN = 4096, DH = 128;
constexpr int DEPTH = 8;
constexpr int D0 = 4;
constexpr int GPOS = 18;
constexpr int QPOS = 24;
constexpr unsigned CODE_BYTES = 6784;
constexpr int AUX_NT = 2, AUX_SC1 = 16;
constexpr unsigned RSRC_FLAGS = 0x00020000u;
constexpr unsigned SPIN_CAP = 1u << 18;
constexpr unsigned WS_EPOCH = 0;
constexpr unsigned WS_QP = 4096;
constexpr unsigned WS_OP = WS_QP + 8 * 2 * 4 * 2048;
constexpr unsigned WS_FP = WS_OP + 8 * 2 * 16 * 4096;
constexpr unsigned WS_END = WS_FP + 4 * 64 * 128 * 16;
constexpr int L_W = 0;
constexpr int L_MRG = 16384;
constexpr int L_Q = L_MRG + 4 * 4 * 128 * 4;
constexpr int L_MAX = L_Q + 1024;
constexpr int L_SUM = L_MAX + 64;
constexpr int L_O = L_SUM + 64;
constexpr int L_X = L_O + 256;
constexpr int L_END = L_X + 512;

__device__ __forceinline__ v4f mfma16(v4u a, v4u b, v4f c) {
  return __builtin_amdgcn_mfma_f32_16x16x32_bf16(__builtin_bit_cast(v8bf, a), __builtin_bit_cast(v8bf, b), c, 0, 0, 0);
}
__device__ __forceinline__ unsigned pkbf(float lo, float hi) {
  v2bf r; r[0] = (__bf16)lo; r[1] = (__bf16)hi; return __builtin_bit_cast(unsigned, r);
}
template <int CTRL> __device__ __forceinline__ float dpp(float x) {
  return __builtin_bit_cast(float, __builtin_amdgcn_mov_dpp(__builtin_bit_cast(int, x), CTRL, 0xf, 0xf, true));
}
constexpr int SHL4 = 0x104, SHL8 = 0x108, SHL12 = 0x10c, SHR4 = 0x114, SHR8 = 0x118, SHR12 = 0x11c, ROR8 = 0x128;
__device__ __forceinline__ float vmax(float a, float b) { return __builtin_elementwise_maximum(a, b); }
__device__ __forceinline__ float fold32(float a, float b) { auto t = __builtin_amdgcn_permlane32_swap(U(a), U(b), false, false); return F(t[0]) + F(t[1]); }
__device__ __forceinline__ float fold16(float a, float b) { auto t = __builtin_amdgcn_permlane16_swap(U(a), U(b), false, false); return F(t[0]) + F(t[1]); }
__device__ __forceinline__ float allmax32(float a) { auto t = __builtin_amdgcn_permlane32_swap(U(a), U(a), false, false); return vmax(F(t[0]), F(t[1])); }
__device__ __forceinline__ float allmax16(float a) { auto t = __builtin_amdgcn_permlane16_swap(U(a), U(a), false, false); return vmax(F(t[0]), F(t[1])); }
__device__ __forceinline__ v4u tr_frag(LAS unsigned char* p, int second_off) {
  const v4s a = __builtin_amdgcn_ds_read_tr16_b64_v4i16((LAS v4s*)p);
  const v4s b = __builtin_amdgcn_ds_read_tr16_b64_v4i16((LAS v4s*)(p + second_off));
  const v2u a2 = __builtin_bit_cast(v2u, a), b2 = __builtin_bit_cast(v2u, b);
  return v4u{a2[0], a2[1], b2[0], b2[1]};
}
__device__ __forceinline__ void lds_barrier() {
  SB();
  asm volatile("s_waitcnt lgkmcnt(0)" ::: "memory");
  __builtin_amdgcn_s_barrier();
  MEMBAR();
  SB();
}
__device__ __forceinline__ unsigned badtag(v4u d, unsigned tag) { return (d[1] ^ tag) | (d[3] ^ tag); }
template <unsigned RND>
__device__ __forceinline__ v4u pack4(v4f a, unsigned tag) {
  const unsigned r0 = U(a[0]) + RND, r1 = U(a[1]) + RND, r2 = U(a[2]) + RND, r3 = U(a[3]) + RND;
  return v4u{__builtin_amdgcn_perm(r0, r1, 0x07060503u), __builtin_amdgcn_perm(r1, tag, 0x06050100u),
             __builtin_amdgcn_perm(r2, r3, 0x07060503u), __builtin_amdgcn_perm(r3, tag, 0x06050100u)};
}
__device__ __forceinline__ v4f unpack4(v4u d) {
  return v4f{F(d[0] & 0xffffff00u), F(__builtin_amdgcn_perm(d[0], d[1], 0x0403020cu)), F(d[2] & 0xffffff00u), F(__builtin_amdgcn_perm(d[2], d[3], 0x0403020cu))};
}
__device__ __forceinline__ unsigned sel(unsigned long long m, unsigned t, unsigned f) { return __builtin_amdgcn_inverse_ballot_w64(m) ? t : f; }

__global__ void __launch_bounds__(512, 2)
decode_fused(const void* x_, const void* kc_, const void* vc_, const void* wq_, const void* wo_, void* out_, void* ws_, unsigned long long pad_) {
  __shared__ __attribute__((aligned(16))) unsigned char lds_raw[L_END];
  LAS unsigned char* lds = (LAS unsigned char*)lds_raw;
  v8q ka;
  unsigned long long pc0;
  unsigned ka_early;
  asm volatile("s_load_dword %1, %2, 0x0\n\ts_getpc_b64 %0" : "=&s"(pc0), "=&s"(ka_early) : "s"(__builtin_amdgcn_kernarg_segment_ptr()));
  SB();
  const unsigned cpf = __builtin_amdgcn_raw_buffer_load_b32(__builtin_amdgcn_make_buffer_rsrc((void*)pc0, 0, blockIdx.x < 16 ? CODE_BYTES : 0, RSRC_FLAGS),
                                                            (threadIdx.x - 448u) * 128u, 0, 0);
  SB();
  const int w = __builtin_amdgcn_readfirstlane(threadIdx.x >> 6), l = threadIdx.x & 63;
  const int bid = blockIdx.x, g = bid & 7, j = bid >> 3, b = j >> 4, s = j & 15;
  const int g4 = l >> 4, bank = (l >> 2) & 3;
  const int gn = (g + 1) & 7;
  const int rq = j >> 3, cq = j & 7;
  const int r8 = j >> 2, hh = r8 >> 1, half = r8 & 1, c = j & 3;
  __amdgpu_buffer_rsrc_t rws;
  unsigned tag, t0 = 0, t1 = 0, t2 = 0;
  unsigned v_h1, s_h1, a_ow, a_or, a_tb, s_fp;
  auto tail_addresses = [&]() __attribute__((always_inline)) {
    v_h1 = (l >> 3) * 4096 + 16 * (l & 7);
    s_h1 = WS_OP + ((gn * 2 + (w >> 2)) * 16 * 4 + hh) * 1024 + (4 * half + (w & 3)) * 128;
    a_ow = L_O + (w >> 2) * 128 + (16 * (w & 3) + 4 * (l & 3) + 2 * (l >> 5) + (g4 & 1)) * 2;
    a_or = L_O + (l & 1) * 128 + 16 * g4;
    a_tb = L_W + (8 * g4 + bank) * 256 + 8 * (l & 3) + 32 * w;
    s_fp = WS_FP + ((c * 64 + gn * 8 + r8) * 128 + 16 * w) * 16;
    asm volatile("" : "+v"(v_h1), "+v"(a_ow), "+v"(a_or), "+v"(a_tb));
    asm volatile("" : "+s"(s_h1), "+s"(s_fp));
  };

  if (__builtin_expect(w < 4, 1)) {
    asm volatile("s_load_dwordx16 %0, %1, 0x0" : "=s"(ka) : "s"(__builtin_amdgcn_kernarg_segment_ptr()));
    SB();
    unsigned wq_v = (l >> 3) * 8192 + 16 * (l & 7), wq_s = (128 * rq + 32 * w) * 8192 + gn * 1024 + 128 * cq;
    unsigned x_v = (l & 4) * 256 + 16 * (l & 3), x_s = 256 * rq + 64 * w;
    unsigned koff = 256 * bank + 64 * g4 + 16 * (l & 3);
    unsigned long long kvoff = ((unsigned long long)(b * 8 + g) * SK + (SK - WIN) + s * 256) * DH * 2;
    asm volatile("" : "+v"(wq_v), "+v"(x_v), "+v"(koff));
    asm volatile("" : "+s"(wq_s), "+s"(x_s), "+s"(kvoff));
    SB();
    asm volatile("s_waitcnt lgkmcnt(0)" : "+s"(ka) : "s"(ka_early));
    rws = __builtin_amdgcn_make_buffer_rsrc((void*)ka[6], 0, WS_END, RSRC_FLAGS);
    const __amdgpu_buffer_rsrc_t rq_ = __builtin_amdgcn_make_buffer_rsrc((void*)ka[3], 0, 512 * 4096 * 2, RSRC_FLAGS);
    const __amdgpu_buffer_rsrc_t rx = __builtin_amdgcn_make_buffer_rsrc((void*)ka[0], 0, 2 * 512 * 2, RSRC_FLAGS);
    v4u wv[4], kk[16], vv[16];
#pragma unroll
    for (int i = 0; i < 4; ++i) { wv[i] = __builtin_amdgcn_raw_buffer_load_b128(rq_, wq_v, wq_s + i * 65536, AUX_NT); SB(); }
    const v4u xv = __builtin_amdgcn_raw_buffer_load_b128(rx, x_v, x_s, 0);
    SB();
    const __amdgpu_buffer_rsrc_t rk = __builtin_amdgcn_make_buffer_rsrc((void*)(ka[1] + kvoff), 0, 65536, RSRC_FLAGS);
    const __amdgpu_buffer_rsrc_t rv = __builtin_amdgcn_make_buffer_rsrc((void*)(ka[2] + kvoff), 0, 65536, RSRC_FLAGS);
    const int rot = s + 5 * g + 3 * b;
    auto stream = [&](int m) __attribute__((always_inline)) {
      const unsigned blk = (((m + rot) & 15) * 4 + w) * 1024;
      if (m < 16) kk[m] = __builtin_amdgcn_raw_buffer_load_b128(rk, koff, blk, AUX_NT);
      else vv[m - 16] = __builtin_amdgcn_raw_buffer_load_b128(rv, 16 * l, blk, AUX_NT);
      SB();
    };
    __builtin_amdgcn_s_barrier();
    SB();
#pragma unroll
    for (int m = 0; m < D0; ++m) stream(m);
    tag = *(const CAS unsigned*)(ka[6] + WS_EPOCH) + 1u;

#pragma unroll
    for (int i = 0; i < 4; ++i) { *(LAS v4u*)(lds + L_W + (4 * w + i) * 1024 + 16 * l) = wv[i]; SB(); stream(D0 + i); }
    *(LAS v4u*)(lds + L_X + (l & 4) * 64 + 64 * w + 16 * (l & 3)) = xv;
    asm volatile("" :: "v"(cpf));
    SB();
    stream(D0 + 4);
    lds_barrier();
    {
      LAS unsigned char* tb = lds + L_W + (8 * g4 + bank) * 128 + 8 * (l & 3) + 32 * w;
      v4u f[4], xa[4];
#pragma unroll
      for (int ks = 0; ks < 4; ++ks) { f[ks] = tr_frag(tb + ks * 32 * 128, 4 * 128); xa[ks] = *(LAS v4u*)(lds + L_X + (l & 1) * 256 + 64 * ks + 16 * g4); }
      SB();
      v4f qa = mfma16(f[0], xa[0], v4f{0.f, 0.f, 0.f, 0.f}), qb = mfma16(f[1], xa[1], v4f{0.f, 0.f, 0.f, 0.f});
      qa = mfma16(f[2], xa[2], qa); qb = mfma16(f[3], xa[3], qb);
      if ((l & 15) < 2) *(LAS v4u*)(lds + L_MRG + (l & 1) * 256 + 64 * w + 16 * g4) = pack4<0x80u>(qa + qb, tag);
    }
    lds_barrier();
    tail_addresses();
    unsigned a_qf = L_Q + (l & 3) * 256 + 16 * (4 * g4 + bank);
    unsigned a_mw = L_MRG + (w * 4 * 128 + 8 * (l & 15) + 2 * g4) * 4, a_mr = L_MRG + (w * 128 + 16 * (l >> 3) + 4 * (l & 3)) * 4, a_ls = L_SUM + 4 * w;
    unsigned s_op = WS_OP + (((g * 2 + b) * 16 + s) * 4 + w) * 1024;
    unsigned long long m_b0 = __ballot(bank == 0), m_ml = __ballot(l & 4);
    asm volatile("" : "+v"(a_qf), "+v"(a_mw), "+v"(a_mr), "+v"(a_ls));
    asm volatile("" : "+s"(s_op), "+s"(m_b0), "+s"(m_ml));

    v4u qg[2];
    auto gather = [&]() __attribute__((always_inline)) {
      MEMBAR();
#pragma unroll
      for (int i = 0; i < 2; ++i)
        qg[i] = __builtin_amdgcn_raw_buffer_load_b128(rws, (l >> 5) * 2048 + 16 * (l & 31), WS_QP + (g * 2 + b) * 8192 + i * 4096 + w * 512, AUX_SC1);
      SB();
    };
    auto gather_bad = [&]() __attribute__((always_inline)) -> bool { return !__all(((badtag(qg[0], tag) | badtag(qg[1], tag)) & 0xffffu) == 0); };
#define STEP(m) if constexpr ((m) >= D0 + 5 && (m) < QPOS) { \
      if constexpr ((m) == GPOS) gather(); \
      WAITVM(DEPTH - 1 + (((m) >= GPOS && (m) - DEPTH < GPOS) ? 2 : 0)); \
      stream(m); }
#define STEP4(m) STEP(m) STEP(m + 1) STEP(m + 2) STEP(m + 3)
    STEP4(4) STEP4(8) STEP4(12) STEP4(16) STEP4(20) STEP4(24) STEP4(28)
#define LATE(k) if constexpr (QPOS + (k) < 32) { SB(); stream(QPOS + (k)); }
    if (gather_bad()) {
#pragma unroll 1
      for (unsigned spin = 0; spin < SPIN_CAP; ++spin) { gather(); if (!gather_bad()) break; }
    }
    {
      const v4f a = unpack4(qg[0]) + unpack4(qg[1]);
      const float e0 = fold32(a[0], a[2]), e1 = fold32(a[1], a[3]);
      *(LAS unsigned*)(lds + L_Q + w * 256 + 8 * (l & 31) + 4 * (l >> 5)) = pkbf(e0, e1);
    }
    LATE(0)
    lds_barrier();

    const v4u qf = *(LAS v4u*)(lds + a_qf);
    float sc[16];
    {
      v4f dq[16];
#pragma unroll
      for (int i = 0; i < 16; ++i) { dq[i] = mfma16(kk[i], qf, v4f{0.f, 0.f, 0.f, 0.f}); if (i == 7) { LATE(1) } }
      LATE(2)
#pragma unroll
      for (int i = 0; i < 16; ++i) sc[i] = dq[i][0] + dpp<SHL4>(dq[i][1]);
      LATE(3)
#pragma unroll
      for (int i = 0; i < 16; ++i) sc[i] += dpp<SHL8>(dq[i][2]);
      LATE(4)
#pragma unroll
      for (int i = 0; i < 16; ++i) sc[i] += dpp<SHL12>(dq[i][3]);
      PIN8(sc); PIN8((sc + 8));
      LATE(5)
    }
    {
      unsigned u[8];
#pragma unroll
      for (int i = 0; i < 8; ++i) u[i] = pkbf(sc[2 * i], sc[2 * i + 1]);
      PIN8(u);
#pragma unroll
      for (int i = 0; i < 8; ++i) { const v2f t = v2f{F(u[i] << 16), F(u[i] & 0xffff0000u)} * 0.08838834764831845f; sc[2 * i] = t[0]; sc[2 * i + 1] = t[1]; }
      SB();
#pragma unroll
      for (int i = 0; i < 8; ++i) u[i] = pkbf(sc[2 * i], sc[2 * i + 1]);
      PIN8(u);
#pragma unroll
      for (int i = 0; i < 8; ++i) { sc[2 * i] = F(u[i] << 16); sc[2 * i + 1] = F(u[i] & 0xffff0000u); }
    }
    float mx = sc[0];
#pragma unroll
    for (int i = 1; i < 16; ++i) mx = vmax(mx, sc[i]);
    LATE(6)
    mx = allmax32(allmax16(mx));
    if (l < 4) *(LAS float*)(lds + L_MAX + (w * 4 + l) * 4) = mx;
    LATE(7)
    lds_barrier();
    constexpr float CS = 1.4426950408889634f;
    const LAS float* pm = (const LAS float*)(lds + L_MAX) + (l & 3);
    const float M2 = vmax(vmax(pm[0], pm[4]), vmax(pm[8], pm[12])) * CS;
#pragma unroll
    for (int i = 0; i < 16; i += 2) {
      const v2f t = v2f{sc[i], sc[i + 1]} * CS - M2;
      sc[i] = __builtin_amdgcn_exp2f(t[0]); sc[i + 1] = __builtin_amdgcn_exp2f(t[1]);
    }
    v4u pf[2][4];
#pragma unroll
    for (int u = 0; u < 2; ++u)
#pragma unroll
      for (int m = 0; m < 4; ++m) pf[u][0][m] = sel(m_b0, pkbf(sc[8 * u + 2 * m], sc[8 * u + 2 * m + 1]), 0u);
    const v4u ones = {0x3f803f80u, 0x3f803f80u, 0x3f803f80u, 0x3f803f80u};
    const v4f lsum = mfma16(pf[1][0], ones, mfma16(pf[0][0], ones, v4f{0.f, 0.f, 0.f, 0.f}));
#pragma unroll
    for (int u = 0; u < 2; ++u)
#pragma unroll
      for (int m = 0; m < 4; ++m) {
        pf[u][1][m] = U(dpp<SHR4>(F(pf[u][0][m])));
        pf[u][2][m] = U(dpp<SHR8>(F(pf[u][0][m])));
        pf[u][3][m] = U(dpp<SHR12>(F(pf[u][0][m])));
      }
    if (l == 0) *(LAS v4f*)(lds + L_SUM + 16 * w) = lsum;
    const unsigned Mw = __builtin_amdgcn_readlane(U(M2), w);

    v4f acc[2] = {v4f{0.f, 0.f, 0.f, 0.f}, v4f{0.f, 0.f, 0.f, 0.f}};
#pragma unroll
    for (int u = 0; u < 2; ++u) {
      v4u vt[8];
#pragma unroll
      for (int m = 0; m < 4; ++m) {
#pragma unroll
        for (int d = 0; d < 8; ++d) vt[d][m] = __builtin_amdgcn_perm(vv[8 * u + 2 * m + 1][d >> 1], vv[8 * u + 2 * m][d >> 1], (d & 1) ? 0x07060302u : 0x05040100u);
        if (m < 3) SB();
      }
#pragma unroll
      for (int d = 0; d < 8; ++d) acc[d & 1] = mfma16(pf[u][d >> 1], vt[d], acc[d & 1]);
      SB();
    }
#pragma unroll
    for (int h = 0; h < 4; ++h) *(LAS v2f*)(lds + a_mw + h * 512) = v2f{acc[0][h], acc[1][h]};
    lds_barrier();
    {
      const LAS float* pl = (const LAS float*)(lds + a_ls);
      const v4f ps = {pl[0], pl[4], pl[8], pl[12]};
      const LAS v4f* src = (const LAS v4f*)(lds + a_mr);
      const v4f o0 = src[0], o1 = src[128], o2 = src[256], o3 = src[384];
      SB();
      const v4u pk = pack4<0u>((o0 + o1) + (o2 + o3), tag);
      __builtin_amdgcn_raw_buffer_store_b128(v4u{sel(m_ml, Mw, pk[0]), pk[1], sel(m_ml, U((ps[0] + ps[1]) + (ps[2] + ps[3])), pk[2]), pk[3]}, rws, 16 * l, s_op, AUX_SC1);
    }
    WAITVM(0);
  } else {
    SB();
    __builtin_amdgcn_s_barrier();
    SB();
    asm volatile("s_load_dwordx16 %0, %1, 0x0\n\ts_waitcnt lgkmcnt(0)" : "=&s"(ka) : "s"(__builtin_amdgcn_kernarg_segment_ptr()), "s"(ka_early));
    rws = __builtin_amdgcn_make_buffer_rsrc((void*)ka[6], 0, WS_END, RSRC_FLAGS);
    tag = *(const CAS unsigned*)(ka[6] + WS_EPOCH) + 1u;
    tail_addresses();
    __builtin_amdgcn_s_barrier();
    lds_barrier();
    if (w == 4 && l < 32)
      __builtin_amdgcn_raw_buffer_store_b128(*(LAS v4u*)(lds + L_MRG + 16 * l), rws, (l >> 4) * 8192 + 16 * (l & 15), WS_QP + (gn * 8 + rq) * 2048 + 256 * cq, AUX_SC1);
    lds_barrier();
    __builtin_amdgcn_s_barrier();
    const unsigned wo_s = (gn * 512 + 64 * r8 + 16 * (w & 3)) * 1024 + 256 * c;
    t0 = __builtin_amdgcn_raw_buffer_load_b32(rws, (l & 3) * 128, WS_FP + ((c * 64 + gn * 8 + r8) * 128 + 32 * (w - 4)) * 16, 0);
    t1 = __builtin_amdgcn_raw_buffer_load_b32(__builtin_amdgcn_make_buffer_rsrc((void*)ka[5], 0, (w == 4 && j < 8) ? 2048 : 0, RSRC_FLAGS), (l & 1) * 1024, (g * 8 + j) * 16, 0);
    t2 = __builtin_amdgcn_raw_buffer_load_b32(__builtin_amdgcn_make_buffer_rsrc((void*)ka[4], 0, w == 5 ? 4096 * 512 * 2 : 0, RSRC_FLAGS), 0, wo_s, AUX_NT);
    asm volatile("" :: "v"(cpf));
    SB();
    __builtin_amdgcn_s_barrier();
    const __amdgpu_buffer_rsrc_t ro = __builtin_amdgcn_make_buffer_rsrc((void*)ka[4], 0, 4096 * 512 * 2, RSRC_FLAGS);
#pragma unroll
    for (int i = 0; i < 4; ++i)
      __builtin_amdgcn_raw_ptr_buffer_load_lds(ro, lds + L_W + (w & 3) * 4096 + i * 1024, 16, g4 * 1024 + 16 * (l & 15), wo_s + i * 4096, 0, AUX_NT);
    SB();
  }

  __builtin_amdgcn_s_barrier();
  SB();
  v4u wf[2];
  {
    v4u d[2];
    auto poll = [&]() __attribute__((always_inline)) {
      MEMBAR();
#pragma unroll
      for (int i = 0; i < 2; ++i) d[i] = __builtin_amdgcn_raw_buffer_load_b128(rws, v_h1, s_h1 + i * 32768, AUX_SC1);
      SB();
    };
    auto poll_bad = [&]() __attribute__((always_inline)) -> bool { return !__all(((badtag(d[0], tag) | badtag(d[1], tag)) & 0xffffu) == 0); };
    poll();
    asm volatile("s_waitcnt vmcnt(2)" ::: "memory");
    asm volatile("" :: "v"(t0), "v"(t1), "v"(t2));
    lds_barrier();
#pragma unroll
    for (int ks = 0; ks < 2; ++ks) wf[ks] = tr_frag(lds + a_tb + ks * 32 * 256, 4 * 256);
    SB();
    if (poll_bad()) {
#pragma unroll 1
      for (unsigned spin = 0; spin < SPIN_CAP; ++spin) { poll(); if (!poll_bad()) break; }
    }
    const float m0 = dpp<SHL4>(F(d[0][0])), m1 = dpp<SHL4>(F(d[1][0])), l0 = dpp<SHL4>(F(d[0][2])), l1 = dpp<SHL4>(F(d[1][2]));
    float Mx = allmax16(allmax32(vmax(m0, m1)));
    Mx = vmax(Mx, dpp<ROR8>(Mx));
    const float w0 = __builtin_amdgcn_exp2f(m0 - Mx), w1 = __builtin_amdgcn_exp2f(m1 - Mx);
    const v4f a = unpack4(d[0]) * w0 + unpack4(d[1]) * w1;
    float el = l0 * w0 + l1 * w1;
    const float u0 = fold32(a[0], a[2]), u1 = fold32(a[1], a[3]);
    el = fold32(el, el);
    float e = fold16(u0, u1);
    el = fold16(el, el);
    e += dpp<ROR8>(e);
    el += dpp<ROR8>(el);
    e *= __builtin_amdgcn_rcpf(el);
    if (!(l & 12)) *(LAS unsigned short*)(lds + a_ow) = (unsigned short)pkbf(e, e);
  }
  lds_barrier();
  {
    const v4u a0 = *(const LAS v4u*)(lds + a_or), a1 = *(const LAS v4u*)(lds + a_or + 64);
    const v4f f = mfma16(a1, wf[1], mfma16(a0, wf[0], v4f{0.f, 0.f, 0.f, 0.f}));
    if (l < 16) __builtin_amdgcn_raw_buffer_store_b128(v4u{U(f[0]), tag, U(f[1]), tag}, rws, 16 * l, s_fp, AUX_SC1);
  }
  if (j < 8) {
    const int f = g * 8 + j, fc = f >> 4, col0 = 8 * (f & 15);
    const unsigned vo = (l >> 3) * 2048 + 16 * (l & 7);
    const unsigned so = WS_FP + ((fc * 64 + 8 * w) * 128 + col0) * 16;
    v4u d;
    WAITVM(0);
#pragma unroll 1
    for (unsigned spin = 0; spin < SPIN_CAP; ++spin) {
      MEMBAR();
      d = __builtin_amdgcn_raw_buffer_load_b128(rws, vo, so, AUX_SC1);
      if (__all(badtag(d, tag) == 0)) break;
    }
    float r = fold32(F(d[0]), F(d[2]));
    r = fold16(r, r);
    r += dpp<ROR8>(r);
    *(LAS float*)(lds + L_MRG + ((l >> 5) * 8 + (l & 7)) * 32 + 4 * w) = r;
    lds_barrier();
    if (w == 0 && l < 16) {
      const v4f t = *(LAS v4f*)(lds + L_MRG + 32 * l) + *(LAS v4f*)(lds + L_MRG + 32 * l + 16);
      const float z = (t[0] + t[1]) + (t[2] + t[3]);
      ((GAS unsigned short*)ka[5])[(l >> 3) * 512 + 128 * fc + col0 + (l & 7)] = (unsigned short)pkbf(z, z);
      if (f == 0 && l == 0) __builtin_amdgcn_raw_buffer_store_b32(tag, rws, 0, WS_EPOCH, AUX_SC1);
    }
  }
}

extern "C" void kernel_launch(void* const* d_in, const int* in_sizes, int n_in, void* d_out, int out_size, void* d_ws, size_t ws_size,
                              hipStream_t stream) {
  (void)in_sizes; (void)n_in; (void)out_size; (void)ws_size;
  decode_fused<<<256, 512, 0, stream>>>(d_in[0], d_in[1], d_in[2], d_in[3], d_in[4], d_out, d_ws, 0ull);
}
```

```cpp
#define PROBE 0
#define PROBEK 2
#include <hip/hip_runtime.h>
#include <hip/hip_bf16.h>

typedef unsigned v4u __attribute__((ext_vector_type(4)));
typedef unsigned v2u __attribute__((ext_vector_type(2)));
typedef float v4f __attribute__((ext_vector_type(4)));
typedef float v2f __attribute__((ext_vector_type(2)));
typedef short v4s __attribute__((ext_vector_type(4)));
typedef __bf16 v8bf __attribute__((ext_vector_type(8)));
typedef __bf16 v2bf __attribute__((ext_vector_type(2)));
typedef unsigned long long v8q __attribute__((ext_vector_type(8)));
#define LAS __attribute__((address_space(3)))
#define GAS __attribute__((address_space(1)))
#define CAS __attribute__((address_space(4)))
#define SB() __builtin_amdgcn_sched_barrier(0)
#define MEMBAR() asm volatile("" ::: "memory")
#define WAITVM(n) do { SB(); __builtin_amdgcn_s_waitcnt((((n) & 15) | (((n) >> 4) << 14) | (7 << 4) | (15 << 8))); SB(); } while (0)

#define DEPTH 8
#define D0 4
#define GPOS 18
#define QPOS 24
#define WOAT 5
#define NOB7 0
#define CODEWG 16
#define CODEAUX 0
#define CODETOP 1
#define CODEPF 6656
#define ROT (s + 5 * g + 3 * b)
#define ILV 1
#define RJ0 0
#define VROT 0
#define XORM 0
#define LATEUSE 1
#define PRE_AT 4
#define EPAUX 16
#define PUBW 4
constexpr int SK = 32768, WIN = 4096, DH = 128;
constexpr int AUX_NT = 2, AUX_SC1 = 16;
constexpr unsigned RSRC_FLAGS = 0x00020000u;
constexpr unsigned WS_EPOCH = 0;
constexpr unsigned WS_QP = 4096;
constexpr unsigned WS_OP = WS_QP + 8 * 2 * 4 * 2048;
constexpr unsigned WS_FP = WS_OP + 8 * 2 * 16 * 4096;
constexpr unsigned WS_END = WS_FP + 4 * 64 * 128 * 16;
constexpr unsigned WS_STAMP = 4u << 20;
constexpr int L_W = 0;
constexpr int L_MRG = 16384;
constexpr int L_Q = L_MRG + 4 * 4 * 128 * 4;
constexpr int L_MAX = L_Q + 1024;
constexpr int L_SUM = L_MAX + 64;
constexpr int L_O = L_SUM + 64;
constexpr int L_X = L_O + 256;
constexpr int L_END = L_X + 512;
constexpr unsigned SPIN_CAP = 1u << 18;

__device__ __forceinline__ v4f mfma16(v4u a, v4u b, v4f c) {
  return __builtin_amdgcn_mfma_f32_16x16x32_bf16(__builtin_bit_cast(v8bf, a), __builtin_bit_cast(v8bf, b), c, 0, 0, 0);
}
__device__ __forceinline__ unsigned pkbf(float lo, float hi) {
  v2bf r; r[0] = (__bf16)lo; r[1] = (__bf16)hi; return __builtin_bit_cast(unsigned, r);
}
template <int CTRL> __device__ __forceinline__ float dpp(float x) {
  return __builtin_bit_cast(float, __builtin_amdgcn_mov_dpp(__builtin_bit_cast(int, x), CTRL, 0xf, 0xf, true));
}
constexpr int SHL4 = 0x104, SHL8 = 0x108, SHL12 = 0x10c, SHR4 = 0x114, SHR8 = 0x118, SHR12 = 0x11c, ROR8 = 0x128;
#define F(x) __uint_as_float(x)
#define U(x) __float_as_uint(x)
__device__ __forceinline__ float vmax(float a, float b) { return __builtin_elementwise_maximum(a, b); }
__device__ __forceinline__ float fold32(float a, float b) { auto t = __builtin_amdgcn_permlane32_swap(U(a), U(b), false, false); return F(t[0]) + F(t[1]); }
__device__ __forceinline__ float fold16(float a, float b) { auto t = __builtin_amdgcn_permlane16_swap(U(a), U(b), false, false); return F(t[0]) + F(t[1]); }
__device__ __forceinline__ float foldmax32(float a, float b) { auto t = __builtin_amdgcn_permlane32_swap(U(a), U(b), false, false); return vmax(F(t[0]), F(t[1])); }
__device__ __forceinline__ float foldmax16(float a, float b) { auto t = __builtin_amdgcn_permlane16_swap(U(a), U(b), false, false); return vmax(F(t[0]), F(t[1])); }
__device__ __forceinline__ v4u tr_frag(LAS unsigned char* p, int second_off) {
  const v4s a = __builtin_amdgcn_ds_read_tr16_b64_v4i16((LAS v4s*)p);
  const v4s b = __builtin_amdgcn_ds_read_tr16_b64_v4i16((LAS v4s*)(p + second_off));
  const v2u a2 = __builtin_bit_cast(v2u, a), b2 = __builtin_bit_cast(v2u, b);
  return v4u{a2[0], a2[1], b2[0], b2[1]};
}
__device__ __forceinline__ void lds_barrier() {
  SB();
  asm volatile("s_waitcnt lgkmcnt(0)" ::: "memory");
  __builtin_amdgcn_s_barrier();
  MEMBAR();
  SB();
}
__device__ __forceinline__ unsigned badtag(v4u d, unsigned tag) { return (d[1] ^ tag) | (d[3] ^ tag); }
template <unsigned RND>
__device__ __forceinline__ v4u pack4(v4f a, unsigned tag) {
  const unsigned r0 = U(a[0]) + RND, r1 = U(a[1]) + RND, r2 = U(a[2]) + RND, r3 = U(a[3]) + RND;
  return v4u{__builtin_amdgcn_perm(r0, r1, 0x07060503u), __builtin_amdgcn_perm(r1, tag, 0x06050100u),
             __builtin_amdgcn_perm(r2, r3, 0x07060503u), __builtin_amdgcn_perm(r3, tag, 0x06050100u)};
}
__device__ __forceinline__ v4f unpack4(v4u d) {
  return v4f{F(d[0] & 0xffffff00u), F(__builtin_amdgcn_perm(d[0], d[1], 0x0403020cu)), F(d[2] & 0xffffff00u), F(__builtin_amdgcn_perm(d[2], d[3], 0x0403020cu))};
}
__device__ __forceinline__ unsigned sel(unsigned long long m, unsigned t, unsigned f) { return __builtin_amdgcn_inverse_ballot_w64(m) ? t : f; }
#define STAMP(i) do { SB(); ts[i] = __builtin_amdgcn_s_memrealtime(); SB(); } while (0)
#define COUNT(x) ++x
#define DSTAMP(k) do { SB(); td[k] = (unsigned)__builtin_amdgcn_s_memrealtime(); SB(); } while (0)
#define DUMP() do { if (td[0] && ts[PROBEK]) { const unsigned long long e = __builtin_amdgcn_s_memrealtime() + 8 * ((unsigned)ts[PROBEK] - td[0]); while (__builtin_amdgcn_s_memrealtime() < e) __builtin_amdgcn_s_sleep(2); } } while (0)

__global__ void __launch_bounds__(512, 2)
decode_fused(const void* x_, const void* kc_, const void* vc_, const void* wq_, const void* wo_, void* out_, void* ws_, unsigned long long pad_) {
  __shared__ __attribute__((aligned(16))) unsigned char lds_raw[L_END];
  LAS unsigned char* lds = (LAS unsigned char*)lds_raw;
  unsigned long long ts[16] = {0, 0, 0, 0, 0, 0, 0, 0, 0, 0, 0, 0, 0, 0, 0, 0};
  unsigned rounds0 = 0, rounds1 = 0, rounds2 = 0;
  unsigned td[8] = {0, 0, 0, 0, 0, 0, 0, 0};
  STAMP(0);
  v8q ka;
  unsigned long long pc0;
  unsigned ka_early;
  asm volatile("s_load_dword %1, %2, 0x0\n\ts_getpc_b64 %0" : "=&s"(pc0), "=&s"(ka_early) : "s"(__builtin_amdgcn_kernarg_segment_ptr()));
  SB();
  unsigned cpf;
  {
    unsigned voff = (threadIdx.x - 448u) * 128u;
    cpf = __builtin_amdgcn_raw_buffer_load_b32(__builtin_amdgcn_make_buffer_rsrc((void*)pc0, 0, blockIdx.x < CODEWG ? CODEPF : 0, RSRC_FLAGS), voff, 0, CODEAUX);
  }
  SB();
  const int w = __builtin_amdgcn_readfirstlane(threadIdx.x >> 6), l = threadIdx.x & 63;
  const int bid = blockIdx.x, g = bid & 7, j = bid >> 3, b = j >> 4, s = j & 15;
  const int g4 = l >> 4, bank = (l >> 2) & 3;
  const int gn = (g + 1) & 7;
  const int rq = j >> 3, cq = j & 7;
  const int r8 = j >> 2, hh = r8 >> 1, half = r8 & 1, c = j & 3;
  __amdgpu_buffer_rsrc_t rws;
  unsigned tag, t0 = 0, t1 = 0, t2 = 0;
  unsigned v_h1, s_h1, a_ow, a_or, a_tb, s_fp;
  auto tail_addresses = [&]() __attribute__((always_inline)) {
    v_h1 = (l >> 3) * 4096 + 16 * (l & 7);
    s_h1 = WS_OP + ((gn * 2 + (w >> 2)) * 16 * 4 + hh) * 1024 + (4 * half + (w & 3)) * 128;
    a_ow = L_O + (w >> 2) * 128 + (16 * (w & 3) + 4 * (l & 3) + 2 * (l >> 5) + (g4 & 1)) * 2;
    a_or = L_O + (l & 1) * 128 + 16 * g4;
    a_tb = L_W + (8 * g4 + bank) * 256 + 8 * (l & 3) + 32 * w;
    s_fp = WS_FP + ((c * 64 + gn * 8 + r8) * 128 + 16 * w) * 16;
    asm volatile("" : "+v"(v_h1), "+v"(a_ow), "+v"(a_or), "+v"(a_tb));
    asm volatile("" : "+s"(s_h1), "+s"(s_fp));
  };

  const unsigned wo_s = (gn * 512 + 64 * r8 + 16 * (w & 3)) * 1024 + 256 * c;
  auto issue_wo = [&]() __attribute__((always_inline)) {
    const __amdgpu_buffer_rsrc_t ro = __builtin_amdgcn_make_buffer_rsrc((void*)ka[4], 0, 4096 * 512 * 2, RSRC_FLAGS);
#pragma unroll
    for (int i = 0; i < 4; ++i)
      __builtin_amdgcn_raw_ptr_buffer_load_lds(ro, lds + L_W + (w & 3) * 4096 + i * 1024, 16, g4 * 1024 + 16 * (l & 15), wo_s + i * 4096, 0, AUX_NT);
    SB();
  };
  if (__builtin_expect(w < 4, 1)) {
    asm volatile("s_load_dwordx16 %0, %1, 0x0" : "=s"(ka) : "s"(__builtin_amdgcn_kernarg_segment_ptr()));
    SB();
    unsigned wq_v = (l >> 3) * 8192 + 16 * (l & 7), wq_s = (128 * rq + 32 * w) * 8192 + gn * 1024 + 128 * cq;
    unsigned x_v = (l & 4) * 256 + 16 * (l & 3), x_s = 256 * rq + 64 * w;
    unsigned koff = 256 * bank + 64 * g4 + 16 * (l & 3);
    unsigned long long kvoff = ((unsigned long long)(b * 8 + g) * SK + (SK - WIN) + s * 256) * DH * 2;
    asm volatile("" : "+v"(wq_v), "+v"(x_v), "+v"(koff));
    asm volatile("" : "+s"(wq_s), "+s"(x_s), "+s"(kvoff));
    SB();
    asm volatile("s_waitcnt lgkmcnt(0)" : "+s"(ka) : "s"(ka_early));
    DSTAMP(0);
    rws = __builtin_amdgcn_make_buffer_rsrc((void*)ka[6], 0, WS_END, RSRC_FLAGS);
    const __amdgpu_buffer_rsrc_t rq_ = __builtin_amdgcn_make_buffer_rsrc((void*)ka[3], 0, 512 * 4096 * 2, RSRC_FLAGS);
    const __amdgpu_buffer_rsrc_t rx = __builtin_amdgcn_make_buffer_rsrc((void*)ka[0], 0, 2 * 512 * 2, RSRC_FLAGS);
    v4u wv[4], kk[16], vv[16];
#pragma unroll
    for (int i = 0; i < 4; ++i) { wv[i] = __builtin_amdgcn_raw_buffer_load_b128(rq_, wq_v, wq_s + i * 65536, AUX_NT); SB(); }
    DSTAMP(1);
    const v4u xv = __builtin_amdgcn_raw_buffer_load_b128(rx, x_v, x_s, 0);
    SB();
    DSTAMP(2);
    const __amdgpu_buffer_rsrc_t rk = __builtin_amdgcn_make_buffer_rsrc((void*)(ka[1] + kvoff), 0, 65536, RSRC_FLAGS);
    const __amdgpu_buffer_rsrc_t rv = __builtin_amdgcn_make_buffer_rsrc((void*)(ka[2] + kvoff), 0, 65536, RSRC_FLAGS);
    const int rot = ROT;
    auto stream = [&](int m) __attribute__((always_inline)) {
      const unsigned mm = XORM ? (((m & 15) ^ rot) & 15) : ((m + rot) & 15);
      const unsigned blk = ILV == 1 ? (mm * 4 + w) * 1024 : ILV == 0 ? (w * 16 + mm) * 1024 : ILV == 2 ? ((mm >> 2) * 16 + w * 4 + (mm & 3)) * 1024
                         : ((mm >> 1) * 8 + w * 2 + (mm & 1)) * 1024;
      if (m < 16) kk[m] = __builtin_amdgcn_raw_buffer_load_b128(rk, koff, blk, AUX_NT);
      else vv[m - 16] = __builtin_amdgcn_raw_buffer_load_b128(rv, 16 * l, blk, AUX_NT);
      SB();
    };
    __builtin_amdgcn_s_barrier();
    SB();
    DSTAMP(3);
#pragma unroll
    for (int m = 0; m < D0; ++m) stream(m);
    DSTAMP(4);
    tag = *(const CAS unsigned*)(ka[6] + WS_EPOCH) + 1u;
    STAMP(1);

#pragma unroll
    for (int i = 0; i < 4; ++i) { *(LAS v4u*)(lds + L_W + (4 * w + i) * 1024 + 16 * l) = wv[i]; SB(); if (i == 0) DSTAMP(5); if (i == 3) DSTAMP(6); stream(D0 + i); }
    *(LAS v4u*)(lds + L_X + (l & 4) * 64 + 64 * w + 16 * (l & 3)) = xv;
    SB();
    stream(D0 + 4);
    STAMP(2);
    lds_barrier();
    {
      LAS unsigned char* tb = lds + L_W + (8 * g4 + bank) * 128 + 8 * (l & 3) + 32 * w;
      v4u f[4], xa[4];
#pragma unroll
      for (int ks = 0; ks < 4; ++ks) { f[ks] = tr_frag(tb + ks * 32 * 128, 4 * 128); xa[ks] = *(LAS v4u*)(lds + L_X + (l & 1) * 256 + 64 * ks + 16 * g4); }
      SB();
      v4f qa = mfma16(f[0], xa[0], v4f{0.f, 0.f, 0.f, 0.f}), qb = mfma16(f[1], xa[1], v4f{0.f, 0.f, 0.f, 0.f});
      qa = mfma16(f[2], xa[2], qa); qb = mfma16(f[3], xa[3], qb);
      if ((l & 15) < 2) *(LAS v4u*)(lds + L_MRG + (l & 1) * 256 + 64 * w + 16 * g4) = pack4<0x80u>(qa + qb, tag);
      lds_barrier();
      if (PUBW == 0 && w == 0 && l < 32)
        __builtin_amdgcn_raw_buffer_store_b128(*(LAS v4u*)(lds + L_MRG + 16 * l), rws, (l >> 4) * 8192 + 16 * (l & 15), WS_QP + (gn * 8 + rq) * 2048 + 256 * cq, AUX_SC1);
      SB();
    }
    STAMP(3);
    tail_addresses();
    unsigned a_qf = L_Q + (l & 3) * 256 + 16 * (4 * g4 + bank);
    unsigned a_mw = L_MRG + (w * 4 * 128 + 8 * (l & 15) + 2 * g4) * 4, a_mr = L_MRG + (w * 128 + 16 * (l >> 3) + 4 * (l & 3)) * 4, a_ls = L_SUM + 4 * w;
    unsigned s_op = WS_OP + (((g * 2 + b) * 16 + s) * 4 + w) * 1024;
    unsigned long long m_b0 = __ballot(bank == 0), m_ml = __ballot(l & 4);
    asm volatile("" : "+v"(a_qf), "+v"(a_mw), "+v"(a_mr), "+v"(a_ls));
    asm volatile("" : "+s"(s_op), "+s"(m_b0), "+s"(m_ml));

    v4u qg[2];
    auto gather = [&]() __attribute__((always_inline)) {
      MEMBAR();
#pragma unroll
      for (int i = 0; i < 2; ++i)
        qg[i] = __builtin_amdgcn_raw_buffer_load_b128(rws, (l >> 5) * 2048 + 16 * (l & 31), WS_QP + (g * 2 + b) * 8192 + i * 4096 + w * 512, AUX_SC1);
      SB();
      COUNT(rounds0);
    };
    auto gather_bad = [&]() __attribute__((always_inline)) -> bool { return !__all(((badtag(qg[0], tag) | badtag(qg[1], tag)) & 0xffffu) == 0); };
#define STEP(m) if constexpr ((m) >= D0 + 5 && (m) < QPOS) { \
      if constexpr ((m) == GPOS) gather(); \
      WAITVM(DEPTH - 1 + (((m) >= GPOS && (m) - DEPTH < GPOS) ? 2 : 0)); \
      stream(m); }
#define STEP4(m) STEP(m) STEP(m + 1) STEP(m + 2) STEP(m + 3)
    STEP4(4) STEP4(8) STEP4(12) STEP4(16) STEP4(20) STEP4(24) STEP4(28)
#define LATE(k) if constexpr (QPOS + (k) < 32) { SB(); stream(QPOS + (k)); }
    STAMP(4);
    if (gather_bad()) {
#pragma unroll 1
      for (unsigned spin = 0; spin < SPIN_CAP; ++spin) { gather(); if (!gather_bad()) break; }
    }
    {
      const v4f a = unpack4(qg[0]) + unpack4(qg[1]);
      const float e0 = fold32(a[0], a[2]), e1 = fold32(a[1], a[3]);
      *(LAS unsigned*)(lds + L_Q + w * 256 + 8 * (l & 31) + 4 * (l >> 5)) = pkbf(e0, e1);
    }
    LATE(0)
    lds_barrier();
    STAMP(5);

    const v4u qf = *(LAS v4u*)(lds + a_qf);
    float sc[16];
    {
      v4f dq[16];
#pragma unroll
      for (int i = 0; i < 16; ++i) { dq[i] = mfma16(kk[i], qf, v4f{0.f, 0.f, 0.f, 0.f}); if (i == 7) { LATE(1) } }
      LATE(2)
#pragma unroll
      for (int i = 0; i < 16; ++i) sc[i] = dq[i][0] + dpp<SHL4>(dq[i][1]);
      LATE(3)
#pragma unroll
      for (int i = 0; i < 16; ++i) sc[i] += dpp<SHL8>(dq[i][2]);
      LATE(4)
#pragma unroll
      for (int i = 0; i < 16; ++i) { sc[i] += dpp<SHL12>(dq[i][3]); asm("" : "+v"(sc[i])); }
      LATE(5)
    }
    float mx = sc[0];
#pragma unroll
    for (int i = 1; i < 16; ++i) mx = vmax(mx, sc[i]);
    LATE(6)
    mx = foldmax16(mx, mx);
    mx = foldmax32(mx, mx);
    if (l < 4) *(LAS float*)(lds + L_MAX + (w * 4 + l) * 4) = mx;
    LATE(7) LATE(8) LATE(9) LATE(10) LATE(11) LATE(12) LATE(13) LATE(14) LATE(15)
    lds_barrier();
    DSTAMP(7);
    constexpr float CS = 0.08838834764831845f * 1.4426950408889634f;
    const LAS float* pm = (const LAS float*)(lds + L_MAX) + (l & 3);
    const float M2 = vmax(vmax(pm[0], pm[4]), vmax(pm[8], pm[12])) * CS;
#pragma unroll
    for (int i = 0; i < 16; i += 2) {
      const v2f t = v2f{sc[i], sc[i + 1]} * CS - M2;
      sc[i] = __builtin_amdgcn_exp2f(t[0]); sc[i + 1] = __builtin_amdgcn_exp2f(t[1]);
    }
    v4u pf[2][4];
#pragma unroll
    for (int u = 0; u < 2; ++u)
#pragma unroll
      for (int m = 0; m < 4; ++m) pf[u][0][m] = sel(m_b0, pkbf(sc[8 * u + 2 * m], sc[8 * u + 2 * m + 1]), 0u);
    const v4u ones = {0x3f803f80u, 0x3f803f80u, 0x3f803f80u, 0x3f803f80u};
    const v4f lsum = mfma16(pf[1][0], ones, mfma16(pf[0][0], ones, v4f{0.f, 0.f, 0.f, 0.f}));
#pragma unroll
    for (int u = 0; u < 2; ++u)
#pragma unroll
      for (int m = 0; m < 4; ++m) {
        pf[u][1][m] = U(dpp<SHR4>(F(pf[u][0][m])));
        pf[u][2][m] = U(dpp<SHR8>(F(pf[u][0][m])));
        pf[u][3][m] = U(dpp<SHR12>(F(pf[u][0][m])));
      }
    if (l == 0) *(LAS v4f*)(lds + L_SUM + 16 * w) = lsum;
    const unsigned Mw = __builtin_amdgcn_readlane(U(M2), w);
    STAMP(6);

    v4f acc[2] = {v4f{0.f, 0.f, 0.f, 0.f}, v4f{0.f, 0.f, 0.f, 0.f}};
#pragma unroll
    for (int u = 0; u < 2; ++u) {
      v4u vt[8];
#pragma unroll
      for (int m = 0; m < 4; ++m) {
#pragma unroll
        for (int d = 0; d < 8; ++d) vt[d][m] = __builtin_amdgcn_perm(vv[8 * u + 2 * m + 1][d >> 1], vv[8 * u + 2 * m][d >> 1], (d & 1) ? 0x07060302u : 0x05040100u);
        if (m < 3) SB();
      }
#pragma unroll
      for (int d = 0; d < 8; ++d) acc[d & 1] = mfma16(pf[u][d >> 1], vt[d], acc[d & 1]);
      SB();
    }
    STAMP(7);
#pragma unroll
    for (int h = 0; h < 4; ++h) *(LAS v2f*)(lds + a_mw + h * 512) = v2f{acc[0][h], acc[1][h]};
    lds_barrier();
    {
      const LAS float* pl = (const LAS float*)(lds + a_ls);
      const v4f ps = {pl[0], pl[4], pl[8], pl[12]};
      const LAS v4f* src = (const LAS v4f*)(lds + a_mr);
      const v4f o0 = src[0], o1 = src[128], o2 = src[256], o3 = src[384];
      SB();
      const v4u pk = pack4<0u>((o0 + o1) + (o2 + o3), tag);
      __builtin_amdgcn_raw_buffer_store_b128(v4u{sel(m_ml, Mw, pk[0]), pk[1], sel(m_ml, U((ps[0] + ps[1]) + (ps[2] + ps[3])), pk[2]), pk[3]}, rws, 16 * l, s_op, AUX_SC1);
    }
    STAMP(8);
    while ((unsigned)__builtin_amdgcn_s_memrealtime() - td[0] < 670u) {}
    SB();
    STAMP(14);
  } else {
    SB();
    __builtin_amdgcn_s_barrier();
    SB();
    asm volatile("s_load_dwordx16 %0, %1, 0x0\n\ts_waitcnt lgkmcnt(0)" : "=&s"(ka) : "s"(__builtin_amdgcn_kernarg_segment_ptr()), "s"(ka_early));
    rws = __builtin_amdgcn_make_buffer_rsrc((void*)ka[6], 0, WS_END, RSRC_FLAGS);
    tag = *(const CAS unsigned*)(ka[6] + WS_EPOCH) + 1u;
    tail_addresses();
    __builtin_amdgcn_s_barrier();
    lds_barrier();
    if (PUBW == 4 && w == 4 && l < 32)
      __builtin_amdgcn_raw_buffer_store_b128(*(LAS v4u*)(lds + L_MRG + 16 * l), rws, (l >> 4) * 8192 + 16 * (l & 15), WS_QP + (gn * 8 + rq) * 2048 + 256 * cq, AUX_SC1);
    lds_barrier();
    if (WOAT == 3) issue_wo();
    __builtin_amdgcn_s_barrier();
    t0 = __builtin_amdgcn_raw_buffer_load_b32(rws, (l & 3) * 128, WS_FP + ((c * 64 + gn * 8 + r8) * 128 + 32 * (w - 4)) * 16, 0);
    t1 = __builtin_amdgcn_raw_buffer_load_b32(__builtin_amdgcn_make_buffer_rsrc((void*)ka[5], 0, (w == 4 && (unsigned)(j - RJ0) < 8u) ? 2048 : 0, RSRC_FLAGS), (l & 1) * 1024, (g * 8 + (j & 7)) * 16, 0);
    t2 = __builtin_amdgcn_raw_buffer_load_b32(__builtin_amdgcn_make_buffer_rsrc((void*)ka[4], 0, (w == 5 && WOAT >= 5) ? 4096 * 512 * 2 : 0, RSRC_FLAGS), 0, wo_s, AUX_NT);
    SB();
    if (WOAT == 4) issue_wo();
    asm volatile("" :: "v"(cpf));
    SB();
    __builtin_amdgcn_s_barrier();
    if (WOAT == 5) issue_wo();
  }

  __builtin_amdgcn_s_barrier();
  SB();
  v4u wf[2];
  {
    v4u d[2];
    auto poll = [&]() __attribute__((always_inline)) {
      MEMBAR();
#pragma unroll
      for (int i = 0; i < 2; ++i) d[i] = __builtin_amdgcn_raw_buffer_load_b128(rws, v_h1, s_h1 + i * 32768, AUX_SC1);
      SB();
      COUNT(rounds1);
    };
    auto poll_bad = [&]() __attribute__((always_inline)) -> bool { return !__all(((badtag(d[0], tag) | badtag(d[1], tag)) & 0xffffu) == 0); };
    if (WOAT == 6 && w >= 4) issue_wo();
    poll();
    if (WOAT == 7 && w >= 4) issue_wo();
    asm volatile("s_waitcnt vmcnt(2)" ::: "memory");
    asm volatile("" :: "v"(t0), "v"(t1), "v"(t2));
    STAMP(15);
    lds_barrier();
#pragma unroll
    for (int ks = 0; ks < 2; ++ks) wf[ks] = tr_frag(lds + a_tb + ks * 32 * 256, 4 * 256);
    SB();
    STAMP(9);
    if (poll_bad()) {
#pragma unroll 1
      for (unsigned spin = 0; spin < SPIN_CAP; ++spin) { poll(); if (!poll_bad()) break; }
    }
    STAMP(10);
    const float m0 = dpp<SHL4>(F(d[0][0])), m1 = dpp<SHL4>(F(d[1][0])), l0 = dpp<SHL4>(F(d[0][2])), l1 = dpp<SHL4>(F(d[1][2]));
    float Mx = vmax(m0, m1);
    Mx = foldmax32(Mx, Mx);
    Mx = foldmax16(Mx, Mx);
    Mx = vmax(Mx, dpp<ROR8>(Mx));
    const float w0 = __builtin_amdgcn_exp2f(m0 - Mx), w1 = __builtin_amdgcn_exp2f(m1 - Mx);
    const v4f a = unpack4(d[0]) * w0 + unpack4(d[1]) * w1;
    float el = l0 * w0 + l1 * w1;
    const float t0 = fold32(a[0], a[2]), t1 = fold32(a[1], a[3]);
    el = fold32(el, el);
    float e = fold16(t0, t1);
    el = fold16(el, el);
    e += dpp<ROR8>(e);
    el += dpp<ROR8>(el);
    e *= __builtin_amdgcn_rcpf(el);
    if (!(l & 12)) *(LAS unsigned short*)(lds + a_ow) = (unsigned short)pkbf(e, e);
  }
  lds_barrier();
  {
    const v4u a0 = *(const LAS v4u*)(lds + a_or), a1 = *(const LAS v4u*)(lds + a_or + 64);
    const v4f f = mfma16(a1, wf[1], mfma16(a0, wf[0], v4f{0.f, 0.f, 0.f, 0.f}));
    if (l < 16) __builtin_amdgcn_raw_buffer_store_b128(v4u{U(f[0]), tag, U(f[1]), tag}, rws, 16 * l, s_fp, AUX_SC1);
  }
  STAMP(11);
  if ((unsigned)(j - RJ0) < 8u) {
    const int f = g * 8 + (j & 7), fc = f >> 4, col0 = 8 * (f & 15);
    const unsigned vo = (l >> 3) * 2048 + 16 * (l & 7);
    const unsigned so = WS_FP + ((fc * 64 + 8 * w) * 128 + col0) * 16;
    v4u d;
    WAITVM(0);
#pragma unroll 1
    for (unsigned spin = 0; spin < SPIN_CAP; ++spin) {
      MEMBAR();
      d = __builtin_amdgcn_raw_buffer_load_b128(rws, vo, so, AUX_SC1);
      COUNT(rounds2);
      if (__all(badtag(d, tag) == 0)) break;
    }
    STAMP(12);
    float r = fold32(F(d[0]), F(d[2]));
    r = fold16(r, r);
    r += dpp<ROR8>(r);
    *(LAS float*)(lds + L_MRG + ((l >> 5) * 8 + (l & 7)) * 32 + 4 * w) = r;
    lds_barrier();
    if (w == 0 && l < 16) {
      const v4f t = *(LAS v4f*)(lds + L_MRG + 32 * l) + *(LAS v4f*)(lds + L_MRG + 32 * l + 16);
      const float z = (t[0] + t[1]) + (t[2] + t[3]);
      ((GAS unsigned short*)ka[5])[(l >> 3) * 512 + 128 * fc + col0 + (l & 7)] = (unsigned short)pkbf(z, z);
      if (f == 0 && l == 0) __builtin_amdgcn_raw_buffer_store_b32(tag, rws, 0, WS_EPOCH, EPAUX);
    }
    STAMP(13);
  }
  DUMP();
}

extern "C" void kernel_launch(void* const* d_in, const int* in_sizes, int n_in, void* d_out, int out_size, void* d_ws, size_t ws_size,
                              hipStream_t stream) {
  (void)in_sizes; (void)n_in; (void)out_size; (void)ws_size;
  decode_fused<<<256, 512, 0, stream>>>(d_in[0], d_in[1], d_in[2], d_in[3], d_in[4], d_out, d_ws, 0ull);
}
```

```cpp
#define SKEWSEL 1
#define SKEWBY 1
#include <hip/hip_runtime.h>
#include <hip/hip_bf16.h>

typedef unsigned v4u __attribute__((ext_vector_type(4)));
typedef unsigned v2u __attribute__((ext_vector_type(2)));
typedef float v4f __attribute__((ext_vector_type(4)));
typedef float v2f __attribute__((ext_vector_type(2)));
typedef short v4s __attribute__((ext_vector_type(4)));
typedef __bf16 v8bf __attribute__((ext_vector_type(8)));
typedef __bf16 v2bf __attribute__((ext_vector_type(2)));
typedef unsigned long long v8q __attribute__((ext_vector_type(8)));
#define LAS __attribute__((address_space(3)))
#define GAS __attribute__((address_space(1)))
#define CAS __attribute__((address_space(4)))
#define SB() __builtin_amdgcn_sched_barrier(0)
#define MEMBAR() asm volatile("" ::: "memory")
#define WAITVM(n) do { SB(); __builtin_amdgcn_s_waitcnt((((n) & 15) | (((n) >> 4) << 14) | (7 << 4) | (15 << 8))); SB(); } while (0)
#define PIN8(u) asm("" : "+v"(u[0]), "+v"(u[1]), "+v"(u[2]), "+v"(u[3]), "+v"(u[4]), "+v"(u[5]), "+v"(u[6]), "+v"(u[7]))
#define F(x) __uint_as_float(x)
#define U(x) __float_as_uint(x)

constexpr int SK = 32768, WIN = 4096, DH = 128;
constexpr int DEPTH = 8;
constexpr int D0 = 4;
constexpr int GPOS = 18;
constexpr int QPOS = 24;
constexpr unsigned T_FIRST_LOOK = 670;
constexpr unsigned CODE_BYTES = 6912;
constexpr int AUX_NT = 2, AUX_SC1 = 16;
constexpr unsigned RSRC_FLAGS = 0x00020000u;
constexpr unsigned SPIN_CAP = 1u << 18;
constexpr unsigned WS_EPOCH = 0;
constexpr unsigned WS_QP = 4096;
constexpr unsigned WS_OP = WS_QP + 8 * 2 * 4 * 2048;
constexpr unsigned WS_FP = WS_OP + 8 * 2 * 16 * 4096;
constexpr unsigned WS_TS = WS_FP + 4 * 64 * 128 * 16;
constexpr unsigned WS_END = WS_TS + 256 * 16;
constexpr int L_W = 0;
constexpr int L_MRG = 16384;
constexpr int L_Q = L_MRG + 4 * 4 * 128 * 4;
constexpr int L_MAX = L_Q + 1024;
constexpr int L_SUM = L_MAX + 64;
constexpr int L_O = L_SUM + 64;
constexpr int L_X = L_O + 256;
constexpr int L_END = L_X + 512;

__device__ __forceinline__ v4f mfma16(v4u a, v4u b, v4f c) {
  return __builtin_amdgcn_mfma_f32_16x16x32_bf16(__builtin_bit_cast(v8bf, a), __builtin_bit_cast(v8bf, b), c, 0, 0, 0);
}
__device__ __forceinline__ unsigned pkbf(float lo, float hi) {
  v2bf r; r[0] = (__bf16)lo; r[1] = (__bf16)hi; return __builtin_bit_cast(unsigned, r);
}
template <int CTRL> __device__ __forceinline__ float dpp(float x) {
  return __builtin_bit_cast(float, __builtin_amdgcn_mov_dpp(__builtin_bit_cast(int, x), CTRL, 0xf, 0xf, true));
}
constexpr int SHL4 = 0x104, SHL8 = 0x108, SHL12 = 0x10c, SHR4 = 0x114, SHR8 = 0x118, SHR12 = 0x11c, ROR8 = 0x128;
__device__ __forceinline__ float vmax(float a, float b) { return __builtin_elementwise_maximum(a, b); }
__device__ __forceinline__ float fold32(float a, float b) { auto t = __builtin_amdgcn_permlane32_swap(U(a), U(b), false, false); return F(t[0]) + F(t[1]); }
__device__ __forceinline__ float fold16(float a, float b) { auto t = __builtin_amdgcn_permlane16_swap(U(a), U(b), false, false); return F(t[0]) + F(t[1]); }
__device__ __forceinline__ float allmax32(float a) { auto t = __builtin_amdgcn_permlane32_swap(U(a), U(a), false, false); return vmax(F(t[0]), F(t[1])); }
__device__ __forceinline__ float allmax16(float a) { auto t = __builtin_amdgcn_permlane16_swap(U(a), U(a), false, false); return vmax(F(t[0]), F(t[1])); }
__device__ __forceinline__ v4u tr_frag(LAS unsigned char* p, int second_off) {
  const v4s a = __builtin_amdgcn_ds_read_tr16_b64_v4i16((LAS v4s*)p);
  const v4s b = __builtin_amdgcn_ds_read_tr16_b64_v4i16((LAS v4s*)(p + second_off));
  const v2u a2 = __builtin_bit_cast(v2u, a), b2 = __builtin_bit_cast(v2u, b);
  return v4u{a2[0], a2[1], b2[0], b2[1]};
}
__device__ __forceinline__ void lds_barrier() {
  SB();
  asm volatile("s_waitcnt lgkmcnt(0)" ::: "memory");
  __builtin_amdgcn_s_barrier();
  MEMBAR();
  SB();
}
__device__ __forceinline__ unsigned badtag(v4u d, unsigned tag) { return (d[1] ^ tag) | (d[3] ^ tag); }
template <unsigned RND>
__device__ __forceinline__ v4u pack4(v4f a, unsigned tag) {
  const unsigned r0 = U(a[0]) + RND, r1 = U(a[1]) + RND, r2 = U(a[2]) + RND, r3 = U(a[3]) + RND;
  return v4u{__builtin_amdgcn_perm(r0, r1, 0x07060503u), __builtin_amdgcn_perm(r1, tag, 0x06050100u),
             __builtin_amdgcn_perm(r2, r3, 0x07060503u), __builtin_amdgcn_perm(r3, tag, 0x06050100u)};
}
__device__ __forceinline__ v4f unpack4(v4u d) {
  return v4f{F(d[0] & 0xffffff00u), F(__builtin_amdgcn_perm(d[0], d[1], 0x0403020cu)), F(d[2] & 0xffffff00u), F(__builtin_amdgcn_perm(d[2], d[3], 0x0403020cu))};
}
__device__ __forceinline__ unsigned sel(unsigned long long m, unsigned t, unsigned f) { return __builtin_amdgcn_inverse_ballot_w64(m) ? t : f; }

__global__ void __launch_bounds__(512, 2)
decode_fused(const void* x_, const void* kc_, const void* vc_, const void* wq_, const void* wo_, void* out_, void* ws_, unsigned long long pad_) {
  __shared__ __attribute__((aligned(16))) unsigned char lds_raw[L_END];
  LAS unsigned char* lds = (LAS unsigned char*)lds_raw;
  v8q ka;
  unsigned long long pc0;
  unsigned ka_early;
  asm volatile("s_load_dword %1, %2, 0x0\n\ts_getpc_b64 %0" : "=&s"(pc0), "=&s"(ka_early) : "s"(__builtin_amdgcn_kernarg_segment_ptr()));
  SB();
  const unsigned cpf = __builtin_amdgcn_raw_buffer_load_b32(__builtin_amdgcn_make_buffer_rsrc((void*)pc0, 0, blockIdx.x < 16 ? CODE_BYTES : 0, RSRC_FLAGS),
                                                            (threadIdx.x - 448u) * 128u, 0, 0);
  SB();
  const int w = __builtin_amdgcn_readfirstlane(threadIdx.x >> 6), l = threadIdx.x & 63;
  const int bid = blockIdx.x, g = bid & 7, j = bid >> 3, b = j >> 4, s = j & 15;
  const int g4 = l >> 4, bank = (l >> 2) & 3;
  const int gn = (g + 1) & 7;
  const int rq = j >> 3, cq = j & 7;
  const int r8 = j >> 2, hh = r8 >> 1, half = r8 & 1, c = j & 3;
  __amdgpu_buffer_rsrc_t rws;
  unsigned tag, t0 = 0, t1 = 0, t2 = 0;
  unsigned v_h1, s_h1, a_ow, a_or, a_tb, s_fp;
  auto tail_addresses = [&]() __attribute__((always_inline)) {
    v_h1 = (l >> 3) * 4096 + 16 * (l & 7);
    s_h1 = WS_OP + ((gn * 2 + (w >> 2)) * 16 * 4 + hh) * 1024 + (4 * half + (w & 3)) * 128;
    a_ow = L_O + (w >> 2) * 128 + (16 * (w & 3) + 4 * (l & 3) + 2 * (l >> 5) + (g4 & 1)) * 2;
    a_or = L_O + (l & 1) * 128 + 16 * g4;
    a_tb = L_W + (8 * g4 + bank) * 256 + 8 * (l & 3) + 32 * w;
    s_fp = WS_FP + ((c * 64 + gn * 8 + r8) * 128 + 16 * w) * 16;
    asm volatile("" : "+v"(v_h1), "+v"(a_ow), "+v"(a_or), "+v"(a_tb));
    asm volatile("" : "+s"(s_h1), "+s"(s_fp));
  };

  if (__builtin_expect(w < 4, 1)) {
    asm volatile("s_load_dwordx16 %0, %1, 0x0" : "=s"(ka) : "s"(__builtin_amdgcn_kernarg_segment_ptr()));
    SB();
    unsigned wq_v = (l >> 3) * 8192 + 16 * (l & 7), wq_s = (128 * rq + 32 * w) * 8192 + gn * 1024 + 128 * cq;
    unsigned x_v = (l & 4) * 256 + 16 * (l & 3), x_s = 256 * rq + 64 * w;
    unsigned koff = 256 * bank + 64 * g4 + 16 * (l & 3);
    unsigned long long kvoff = ((unsigned long long)(b * 8 + g) * SK + (SK - WIN) + s * 256) * DH * 2;
    asm volatile("" : "+v"(wq_v), "+v"(x_v), "+v"(koff));
    asm volatile("" : "+s"(wq_s), "+s"(x_s), "+s"(kvoff));
    SB();
    asm volatile("s_waitcnt lgkmcnt(0)" : "+s"(ka) : "s"(ka_early));
    const unsigned t_start = (unsigned)__builtin_amdgcn_s_memrealtime();
    rws = __builtin_amdgcn_make_buffer_rsrc((void*)ka[6], 0, WS_END, RSRC_FLAGS);
    const __amdgpu_buffer_rsrc_t rq_ = __builtin_amdgcn_make_buffer_rsrc((void*)ka[3], 0, 512 * 4096 * 2, RSRC_FLAGS);
    const __amdgpu_buffer_rsrc_t rx = __builtin_amdgcn_make_buffer_rsrc((void*)ka[0], 0, 2 * 512 * 2, RSRC_FLAGS);
    v4u wv[4], kk[16], vv[16];
#pragma unroll
    for (int i = 0; i < 4; ++i) { wv[i] = __builtin_amdgcn_raw_buffer_load_b128(rq_, wq_v, wq_s + i * 65536, AUX_NT); SB(); }
    const v4u xv = __builtin_amdgcn_raw_buffer_load_b128(rx, x_v, x_s, 0);
    SB();
    const __amdgpu_buffer_rsrc_t rk = __builtin_amdgcn_make_buffer_rsrc((void*)(ka[1] + kvoff), 0, 65536, RSRC_FLAGS);
    const __amdgpu_buffer_rsrc_t rv = __builtin_amdgcn_make_buffer_rsrc((void*)(ka[2] + kvoff), 0, 65536, RSRC_FLAGS);
    const int rot = s + 5 * g + 3 * b;
    auto stream = [&](int m) __attribute__((always_inline)) {
      const unsigned blk = (((m + rot) & 15) * 4 + w) * 1024;
      if (m < 16) kk[m] = __builtin_amdgcn_raw_buffer_load_b128(rk, koff, blk, AUX_NT);
      else vv[m - 16] = __builtin_amdgcn_raw_buffer_load_b128(rv, 16 * l, blk, AUX_NT);
      SB();
    };
    __builtin_amdgcn_s_barrier();
    SB();
#pragma unroll
    for (int m = 0; m < D0; ++m) stream(m);
    tag = *(const CAS unsigned*)(ka[6] + WS_EPOCH) + 1u;

#pragma unroll
    for (int i = 0; i < 4; ++i) { *(LAS v4u*)(lds + L_W + (4 * w + i) * 1024 + 16 * l) = wv[i]; SB(); stream(D0 + i); }
    *(LAS v4u*)(lds + L_X + (l & 4) * 64 + 64 * w + 16 * (l & 3)) = xv;
    asm volatile("" :: "v"(cpf));
    SB();
    stream(D0 + 4);
    lds_barrier();
    {
      LAS unsigned char* tb = lds + L_W + (8 * g4 + bank) * 128 + 8 * (l & 3) + 32 * w;
      v4u f[4], xa[4];
#pragma unroll
      for (int ks = 0; ks < 4; ++ks) { f[ks] = tr_frag(tb + ks * 32 * 128, 4 * 128); xa[ks] = *(LAS v4u*)(lds + L_X + (l & 1) * 256 + 64 * ks + 16 * g4); }
      SB();
      v4f qa = mfma16(f[0], xa[0], v4f{0.f, 0.f, 0.f, 0.f}), qb = mfma16(f[1], xa[1], v4f{0.f, 0.f, 0.f, 0.f});
      qa = mfma16(f[2], xa[2], qa); qb = mfma16(f[3], xa[3], qb);
      if ((l & 15) < 2) *(LAS v4u*)(lds + L_MRG + (l & 1) * 256 + 64 * w + 16 * g4) = pack4<0x80u>(qa + qb, tag);
    }
    lds_barrier();
    if (w == 0 && l == 0) __builtin_amdgcn_raw_buffer_store_b128(v4u{t_start, tag, (unsigned)__builtin_amdgcn_s_getreg(20 | (3 << 11)) & 15u, tag}, rws, 0, WS_TS + bid * 16, AUX_SC1);
    tail_addresses();
    unsigned a_qf = L_Q + (l & 3) * 256 + 16 * (4 * g4 + bank);
    unsigned a_mw = L_MRG + (w * 4 * 128 + 8 * (l & 15) + 2 * g4) * 4, a_mr = L_MRG + (w * 128 + 16 * (l >> 3) + 4 * (l & 3)) * 4, a_ls = L_SUM + 4 * w;
    unsigned s_op = WS_OP + (((g * 2 + b) * 16 + s) * 4 + w) * 1024;
    unsigned long long m_b0 = __ballot(bank == 0), m_ml = __ballot(l & 4);
    asm volatile("" : "+v"(a_qf), "+v"(a_mw), "+v"(a_mr), "+v"(a_ls));
    asm volatile("" : "+s"(s_op), "+s"(m_b0), "+s"(m_ml));

    v4u qg[2];
    auto gather = [&]() __attribute__((always_inline)) {
      MEMBAR();
#pragma unroll
      for (int i = 0; i < 2; ++i)
        qg[i] = __builtin_amdgcn_raw_buffer_load_b128(rws, (l >> 5) * 2048 + 16 * (l & 31), WS_QP + (g * 2 + b) * 8192 + i * 4096 + w * 512, AUX_SC1);
      SB();
    };
    auto gather_bad = [&]() __attribute__((always_inline)) -> bool { return !__all(((badtag(qg[0], tag) | badtag(qg[1], tag)) & 0xffffu) == 0); };
#define STEP(m) if constexpr ((m) >= D0 + 5 && (m) < QPOS) { \
      if constexpr ((m) == GPOS) gather(); \
      WAITVM(DEPTH - 1 + (((m) >= GPOS && (m) - DEPTH < GPOS) ? 2 : 0)); \
      stream(m); }
#define STEP4(m) STEP(m) STEP(m + 1) STEP(m + 2) STEP(m + 3)
    STEP4(4) STEP4(8) STEP4(12) STEP4(16) STEP4(20) STEP4(24) STEP4(28)
#define LATE(k) if constexpr (QPOS + (k) < 32) { SB(); stream(QPOS + (k)); }
    if (gather_bad()) {
#pragma unroll 1
      for (unsigned spin = 0; spin < SPIN_CAP; ++spin) { gather(); if (!gather_bad()) break; }
    }
    {
      const v4f a = unpack4(qg[0]) + unpack4(qg[1]);
      const float e0 = fold32(a[0], a[2]), e1 = fold32(a[1], a[3]);
      *(LAS unsigned*)(lds + L_Q + w * 256 + 8 * (l & 31) + 4 * (l >> 5)) = pkbf(e0, e1);
    }
    LATE(0)
    lds_barrier();

    const v4u qf = *(LAS v4u*)(lds + a_qf);
    float sc[16];
    {
      v4f dq[16];
#pragma unroll
      for (int i = 0; i < 16; ++i) { dq[i] = mfma16(kk[i], qf, v4f{0.f, 0.f, 0.f, 0.f}); if (i == 7) { LATE(1) } }
      LATE(2)
#pragma unroll
      for (int i = 0; i < 16; ++i) sc[i] = dq[i][0] + dpp<SHL4>(dq[i][1]);
      LATE(3)
#pragma unroll
      for (int i = 0; i < 16; ++i) sc[i] += dpp<SHL8>(dq[i][2]);
      LATE(4)
#pragma unroll
      for (int i = 0; i < 16; ++i) sc[i] += dpp<SHL12>(dq[i][3]);
      PIN8(sc); PIN8((sc + 8));
      LATE(5)
    }
    {
      unsigned u[8];
#pragma unroll
      for (int i = 0; i < 8; ++i) u[i] = pkbf(sc[2 * i], sc[2 * i + 1]);
      PIN8(u);
#pragma unroll
      for (int i = 0; i < 8; ++i) { const v2f t = v2f{F(u[i] << 16), F(u[i] & 0xffff0000u)} * 0.08838834764831845f; sc[2 * i] = t[0]; sc[2 * i + 1] = t[1]; }
      SB();
#pragma unroll
      for (int i = 0; i < 8; ++i) u[i] = pkbf(sc[2 * i], sc[2 * i + 1]);
      PIN8(u);
#pragma unroll
      for (int i = 0; i < 8; ++i) { sc[2 * i] = F(u[i] << 16); sc[2 * i + 1] = F(u[i] & 0xffff0000u); }
    }
    float mx = sc[0];
#pragma unroll
    for (int i = 1; i < 16; ++i) mx = vmax(mx, sc[i]);
    LATE(6)
    mx = allmax32(allmax16(mx));
    if (l < 4) *(LAS float*)(lds + L_MAX + (w * 4 + l) * 4) = mx;
    LATE(7)
    lds_barrier();
    constexpr float CS = 1.4426950408889634f;
    const LAS float* pm = (const LAS float*)(lds + L_MAX) + (l & 3);
    const float M2 = vmax(vmax(pm[0], pm[4]), vmax(pm[8], pm[12])) * CS;
#pragma unroll
    for (int i = 0; i < 16; i += 2) {
      const v2f t = v2f{sc[i], sc[i + 1]} * CS - M2;
      sc[i] = __builtin_amdgcn_exp2f(t[0]); sc[i + 1] = __builtin_amdgcn_exp2f(t[1]);
    }
    v4u pf[2][4];
#pragma unroll
    for (int u = 0; u < 2; ++u)
#pragma unroll
      for (int m = 0; m < 4; ++m) pf[u][0][m] = sel(m_b0, pkbf(sc[8 * u + 2 * m], sc[8 * u + 2 * m + 1]), 0u);
    const v4u ones = {0x3f803f80u, 0x3f803f80u, 0x3f803f80u, 0x3f803f80u};
    const v4f lsum = mfma16(pf[1][0], ones, mfma16(pf[0][0], ones, v4f{0.f, 0.f, 0.f, 0.f}));
#pragma unroll
    for (int u = 0; u < 2; ++u)
#pragma unroll
      for (int m = 0; m < 4; ++m) {
        pf[u][1][m] = U(dpp<SHR4>(F(pf[u][0][m])));
        pf[u][2][m] = U(dpp<SHR8>(F(pf[u][0][m])));
        pf[u][3][m] = U(dpp<SHR12>(F(pf[u][0][m])));
      }
    if (l == 0) *(LAS v4f*)(lds + L_SUM + 16 * w) = lsum;
    const unsigned Mw = __builtin_amdgcn_readlane(U(M2), w);

    v4f acc[2] = {v4f{0.f, 0.f, 0.f, 0.f}, v4f{0.f, 0.f, 0.f, 0.f}};
#pragma unroll
    for (int u = 0; u < 2; ++u) {
      v4u vt[8];
#pragma unroll
      for (int m = 0; m < 4; ++m) {
#pragma unroll
        for (int d = 0; d < 8; ++d) vt[d][m] = __builtin_amdgcn_perm(vv[8 * u + 2 * m + 1][d >> 1], vv[8 * u + 2 * m][d >> 1], (d & 1) ? 0x07060302u : 0x05040100u);
        if (m < 3) SB();
      }
#pragma unroll
      for (int d = 0; d < 8; ++d) acc[d & 1] = mfma16(pf[u][d >> 1], vt[d], acc[d & 1]);
      SB();
    }
#pragma unroll
    for (int h = 0; h < 4; ++h) *(LAS v2f*)(lds + a_mw + h * 512) = v2f{acc[0][h], acc[1][h]};
    lds_barrier();
    {
      const LAS float* pl = (const LAS float*)(lds + a_ls);
      const v4f ps = {pl[0], pl[4], pl[8], pl[12]};
      const LAS v4f* src = (const LAS v4f*)(lds + a_mr);
      const v4f o0 = src[0], o1 = src[128], o2 = src[256], o3 = src[384];
      SB();
      const v4u pk = pack4<0u>((o0 + o1) + (o2 + o3), tag);
      __builtin_amdgcn_raw_buffer_store_b128(v4u{sel(m_ml, Mw, pk[0]), pk[1], sel(m_ml, U((ps[0] + ps[1]) + (ps[2] + ps[3])), pk[2]), pk[3]}, rws, 16 * l, s_op, AUX_SC1);
    }
    while ((unsigned)__builtin_amdgcn_s_memrealtime() - t_start < T_FIRST_LOOK) {}
    SB();
  } else {
    SB();
    __builtin_amdgcn_s_barrier();
    SB();
    asm volatile("s_load_dwordx16 %0, %1, 0x0\n\ts_waitcnt lgkmcnt(0)" : "=&s"(ka) : "s"(__builtin_amdgcn_kernarg_segment_ptr()), "s"(ka_early));
    rws = __builtin_amdgcn_make_buffer_rsrc((void*)ka[6], 0, WS_END, RSRC_FLAGS);
    tag = *(const CAS unsigned*)(ka[6] + WS_EPOCH) + 1u;
    tail_addresses();
    __builtin_amdgcn_s_barrier();
    lds_barrier();
    if (w == 4 && l < 32)
      __builtin_amdgcn_raw_buffer_store_b128(*(LAS v4u*)(lds + L_MRG + 16 * l), rws, (l >> 4) * 8192 + 16 * (l & 15), WS_QP + (gn * 8 + rq) * 2048 + 256 * cq, AUX_SC1);
    lds_barrier();
    __builtin_amdgcn_s_barrier();
    const unsigned wo_s = (gn * 512 + 64 * r8 + 16 * (w & 3)) * 1024 + 256 * c;
    t0 = __builtin_amdgcn_raw_buffer_load_b32(rws, (l & 3) * 128, WS_FP + ((c * 64 + gn * 8 + r8) * 128 + 32 * (w - 4)) * 16, 0);
    t1 = __builtin_amdgcn_raw_buffer_load_b32(__builtin_amdgcn_make_buffer_rsrc((void*)ka[5], 0, (w == 4 && j < 8) ? 2048 : 0, RSRC_FLAGS), (l & 1) * 1024, (g * 8 + j) * 16, 0);
    t2 = __builtin_amdgcn_raw_buffer_load_b32(__builtin_amdgcn_make_buffer_rsrc((void*)ka[4], 0, w == 5 ? 4096 * 512 * 2 : 0, RSRC_FLAGS), 0, wo_s, AUX_NT);
    asm volatile("" :: "v"(cpf));
    SB();
    __builtin_amdgcn_s_barrier();
    const __amdgpu_buffer_rsrc_t ro = __builtin_amdgcn_make_buffer_rsrc((void*)ka[4], 0, 4096 * 512 * 2, RSRC_FLAGS);
#pragma unroll
    for (int i = 0; i < 4; ++i)
      __builtin_amdgcn_raw_ptr_buffer_load_lds(ro, lds + L_W + (w & 3) * 4096 + i * 1024, 16, g4 * 1024 + 16 * (l & 15), wo_s + i * 4096, 0, AUX_NT);
    SB();
  }

  __builtin_amdgcn_s_barrier();
  SB();
  v4u wf[2];
  {
    v4u d[2];
    auto poll = [&]() __attribute__((always_inline)) {
      MEMBAR();
#pragma unroll
      for (int i = 0; i < 2; ++i) d[i] = __builtin_amdgcn_raw_buffer_load_b128(rws, v_h1, s_h1 + i * 32768, AUX_SC1);
      SB();
    };
    auto poll_bad = [&]() __attribute__((always_inline)) -> bool { return !__all(((badtag(d[0], tag) | badtag(d[1], tag)) & 0xffffu) == 0); };
    poll();
    asm volatile("s_waitcnt vmcnt(2)" ::: "memory");
    asm volatile("" :: "v"(t0), "v"(t1), "v"(t2));
    lds_barrier();
#pragma unroll
    for (int ks = 0; ks < 2; ++ks) wf[ks] = tr_frag(lds + a_tb + ks * 32 * 256, 4 * 256);
    SB();
    if (poll_bad()) {
#pragma unroll 1
      for (unsigned spin = 0; spin < SPIN_CAP; ++spin) { poll(); if (!poll_bad()) break; }
    }
    const float m0 = dpp<SHL4>(F(d[0][0])), m1 = dpp<SHL4>(F(d[1][0])), l0 = dpp<SHL4>(F(d[0][2])), l1 = dpp<SHL4>(F(d[1][2]));
    float Mx = allmax16(allmax32(vmax(m0, m1)));
    Mx = vmax(Mx, dpp<ROR8>(Mx));
    const float w0 = __builtin_amdgcn_exp2f(m0 - Mx), w1 = __builtin_amdgcn_exp2f(m1 - Mx);
    const v4f a = unpack4(d[0]) * w0 + unpack4(d[1]) * w1;
    float el = l0 * w0 + l1 * w1;
    const float u0 = fold32(a[0], a[2]), u1 = fold32(a[1], a[3]);
    el = fold32(el, el);
    float e = fold16(u0, u1);
    el = fold16(el, el);
    e += dpp<ROR8>(e);
    el += dpp<ROR8>(el);
    e *= __builtin_amdgcn_rcpf(el);
    if (!(l & 12)) *(LAS unsigned short*)(lds + a_ow) = (unsigned short)pkbf(e, e);
  }
  lds_barrier();
  {
    const v4u a0 = *(const LAS v4u*)(lds + a_or), a1 = *(const LAS v4u*)(lds + a_or + 64);
    const v4f f = mfma16(a1, wf[1], mfma16(a0, wf[0], v4f{0.f, 0.f, 0.f, 0.f}));
    if (l < 16) __builtin_amdgcn_raw_buffer_store_b128(v4u{U(f[0]), tag, U(f[1]), tag}, rws, 16 * l, s_fp, AUX_SC1);
  }
  if (j < 8) {
    const int f = g * 8 + j, fc = f >> 4, col0 = 8 * (f & 15);
    const unsigned vo = (l >> 3) * 2048 + 16 * (l & 7);
    const unsigned so = WS_FP + ((fc * 64 + 8 * w) * 128 + col0) * 16;
    v4u d;
    WAITVM(0);
#pragma unroll 1
    for (unsigned spin = 0; spin < SPIN_CAP; ++spin) {
      MEMBAR();
      d = __builtin_amdgcn_raw_buffer_load_b128(rws, vo, so, AUX_SC1);
      if (__all(badtag(d, tag) == 0)) break;
    }
    float r = fold32(F(d[0]), F(d[2]));
    r = fold16(r, r);
    r += dpp<ROR8>(r);
    *(LAS float*)(lds + L_MRG + ((l >> 5) * 8 + (l & 7)) * 32 + 4 * w) = r;
    lds_barrier();
    if (w == 0 && l < 16) {
      const v4f t = *(LAS v4f*)(lds + L_MRG + 32 * l) + *(LAS v4f*)(lds + L_MRG + 32 * l + 16);
      const float z = (t[0] + t[1]) + (t[2] + t[3]);
      ((GAS unsigned short*)ka[5])[(l >> 3) * 512 + 128 * fc + col0 + (l & 7)] = (unsigned short)pkbf(z, z);
      if (f == 0 && l == 0) __builtin_amdgcn_raw_buffer_store_b32(tag, rws, 0, WS_EPOCH, AUX_SC1);
    }
    if (w == 1 && f == 0) {
      unsigned mn = 0xffffffffu, mx = 0u, base = 0;
      for (int i = 0; i < 4; ++i) {
        const v4u z = __builtin_amdgcn_raw_buffer_load_b128(rws, 16 * l, WS_TS + i * 1024, AUX_SC1);
        if (i == 0) base = __builtin_amdgcn_readfirstlane(z[0]) - 1000u;
        const unsigned t = z[0] - base;
        mn = t < mn ? t : mn;
        const bool pick = SKEWSEL < 0 ? true : SKEWBY ? ((int)z[2] == SKEWSEL) : (((l + 64 * i) & 7) == SKEWSEL);
        if (pick) mx = t > mx ? t : mx;
      }
      for (int o = 32; o; o >>= 1) { const unsigned a = __shfl_xor(mn, o), c2 = __shfl_xor(mx, o); mn = a < mn ? a : mn; mx = c2 > mx ? c2 : mx; }
      const unsigned long long e = __builtin_amdgcn_s_memrealtime() + 32ull * (unsigned)__builtin_amdgcn_readfirstlane(mx - mn);
      while (__builtin_amdgcn_s_memrealtime() < e) __builtin_amdgcn_s_sleep(2);
    }
  }
}

extern "C" void kernel_launch(void* const* d_in, const int* in_sizes, int n_in, void* d_out, int out_size, void* d_ws, size_t ws_size,
                              hipStream_t stream) {
  (void)in_sizes; (void)n_in; (void)out_size; (void)ws_size;
  decode_fused<<<256, 512, 0, stream>>>(d_in[0], d_in[1], d_in[2], d_in[3], d_in[4], d_out, d_ws, 0ull);
}
```

```cpp
#include <hip/hip_runtime.h>
#include <hip/hip_bf16.h>

typedef unsigned v4u __attribute__((ext_vector_type(4)));
typedef unsigned v2u __attribute__((ext_vector_type(2)));
typedef float v4f __attribute__((ext_vector_type(4)));
typedef float v2f __attribute__((ext_vector_type(2)));
typedef short v4s __attribute__((ext_vector_type(4)));
typedef __bf16 v8bf __attribute__((ext_vector_type(8)));
typedef __bf16 v2bf __attribute__((ext_vector_type(2)));
typedef unsigned long long v8q __attribute__((ext_vector_type(8)));
#define LAS __attribute__((address_space(3)))
#define GAS __attribute__((address_space(1)))
#define CAS __attribute__((address_space(4)))
#define SB() __builtin_amdgcn_sched_barrier(0)
#define MEMBAR() asm volatile("" ::: "memory")
#define WAITVM(n) do { SB(); __builtin_amdgcn_s_waitcnt((((n) & 15) | (((n) >> 4) << 14) | (7 << 4) | (15 << 8))); SB(); } while (0)
#define PIN8(u) asm("" : "+v"(u[0]), "+v"(u[1]), "+v"(u[2]), "+v"(u[3]), "+v"(u[4]), "+v"(u[5]), "+v"(u[6]), "+v"(u[7]))
#define F(x) __uint_as_float(x)
#define U(x) __float_as_uint(x)

constexpr int SK = 32768, WIN = 4096, DH = 128;
constexpr int DEPTH = 8;
constexpr int D0 = 4;
constexpr int GPOS = 18;
constexpr int QPOS = 24;
constexpr unsigned T_FIRST_LOOK = 670;
constexpr unsigned CODE_BYTES = 6912;
constexpr int AUX_NT = 2, AUX_SC1 = 16;
constexpr unsigned RSRC_FLAGS = 0x00020000u;
constexpr unsigned SPIN_CAP = 1u << 18;
constexpr unsigned WS_EPOCH = 0;
constexpr unsigned WS_QP = 4096;
constexpr unsigned WS_OP = WS_QP + 8 * 2 * 4 * 2048;
constexpr unsigned WS_FP = WS_OP + 8 * 2 * 16 * 4096;
constexpr unsigned WS_END = WS_FP + 4 * 64 * 128 * 16;
constexpr int L_W = 0;
constexpr int L_MRG = 16384;
constexpr int L_Q = L_MRG + 4 * 4 * 128 * 4;
constexpr int L_MAX = L_Q + 1024;
constexpr int L_SUM = L_MAX + 64;
constexpr int L_O = L_SUM + 64;
constexpr int L_X = L_O + 256;
constexpr int L_END = L_X + 512;

__device__ __forceinline__ v4f mfma16(v4u a, v4u b, v4f c) {
  return __builtin_amdgcn_mfma_f32_16x16x32_bf16(__builtin_bit_cast(v8bf, a), __builtin_bit_cast(v8bf, b), c, 0, 0, 0);
}
__device__ __forceinline__ unsigned pkbf(float lo, float hi) {
  v2bf r; r[0] = (__bf16)lo; r[1] = (__bf16)hi; return __builtin_bit_cast(unsigned, r);
}
template <int CTRL> __device__ __forceinline__ float dpp(float x) {
  return __builtin_bit_cast(float, __builtin_amdgcn_mov_dpp(__builtin_bit_cast(int, x), CTRL, 0xf, 0xf, true));
}
constexpr int SHL4 = 0x104, SHL8 = 0x108, SHL12 = 0x10c, SHR4 = 0x114, SHR8 = 0x118, SHR12 = 0x11c, ROR8 = 0x128;
__device__ __forceinline__ float vmax(float a, float b) { return __builtin_elementwise_maximum(a, b); }
__device__ __forceinline__ float fold32(float a, float b) { auto t = __builtin_amdgcn_permlane32_swap(U(a), U(b), false, false); return F(t[0]) + F(t[1]); }
__device__ __forceinline__ float fold16(float a, float b) { auto t = __builtin_amdgcn_permlane16_swap(U(a), U(b), false, false); return F(t[0]) + F(t[1]); }
__device__ __forceinline__ float allmax32(float a) { auto t = __builtin_amdgcn_permlane32_swap(U(a), U(a), false, false); return vmax(F(t[0]), F(t[1])); }
__device__ __forceinline__ float allmax16(float a) { auto t = __builtin_amdgcn_permlane16_swap(U(a), U(a), false, false); return vmax(F(t[0]), F(t[1])); }
__device__ __forceinline__ v4u tr_frag(LAS unsigned char* p, int second_off) {
  const v4s a = __builtin_amdgcn_ds_read_tr16_b64_v4i16((LAS v4s*)p);
  const v4s b = __builtin_amdgcn_ds_read_tr16_b64_v4i16((LAS v4s*)(p + second_off));
  const v2u a2 = __builtin_bit_cast(v2u, a), b2 = __builtin_bit_cast(v2u, b);
  return v4u{a2[0], a2[1], b2[0], b2[1]};
}
__device__ __forceinline__ void lds_barrier() {
  SB();
  asm volatile("s_waitcnt lgkmcnt(0)" ::: "memory");
  __builtin_amdgcn_s_barrier();
  MEMBAR();
  SB();
}
__device__ __forceinline__ unsigned badtag(v4u d, unsigned tag) { return (d[1] ^ tag) | (d[3] ^ tag); }
template <unsigned RND>
__device__ __forceinline__ v4u pack4(v4f a, unsigned tag) {
  const unsigned r0 = U(a[0]) + RND, r1 = U(a[1]) + RND, r2 = U(a[2]) + RND, r3 = U(a[3]) + RND;
  return v4u{__builtin_amdgcn_perm(r0, r1, 0x07060503u), __builtin_amdgcn_perm(r1, tag, 0x06050100u),
             __builtin_amdgcn_perm(r2, r3, 0x07060503u), __builtin_amdgcn_perm(r3, tag, 0x06050100u)};
}
__device__ __forceinline__ v4f unpack4(v4u d) {
  return v4f{F(d[0] & 0xffffff00u), F(__builtin_amdgcn_perm(d[0], d[1], 0x0403020cu)), F(d[2] & 0xffffff00u), F(__builtin_amdgcn_perm(d[2], d[3], 0x0403020cu))};
}
__device__ __forceinline__ unsigned sel(unsigned long long m, unsigned t, unsigned f) { return __builtin_amdgcn_inverse_ballot_w64(m) ? t : f; }

__global__ void __launch_bounds__(512, 2)
decode_fused(const void* x_, const void* kc_, const void* vc_, const void* wq_, const void* wo_, void* out_, void* ws_, unsigned long long pad_) {
  __shared__ __attribute__((aligned(16))) unsigned char lds_raw[L_END];
  LAS unsigned char* lds = (LAS unsigned char*)lds_raw;
  v8q ka;
  unsigned long long pc0;
  unsigned ka_early;
  asm volatile("s_load_dword %1, %2, 0x0\n\ts_getpc_b64 %0" : "=&s"(pc0), "=&s"(ka_early) : "s"(__builtin_amdgcn_kernarg_segment_ptr()));
  SB();
  const unsigned cpf = __builtin_amdgcn_raw_buffer_load_b32(__builtin_amdgcn_make_buffer_rsrc((void*)pc0, 0, blockIdx.x < 16 ? CODE_BYTES : 0, RSRC_FLAGS),
                                                            (threadIdx.x - 448u) * 128u, 0, 0);
  SB();
  const int w = __builtin_amdgcn_readfirstlane(threadIdx.x >> 6), l = threadIdx.x & 63;
  const int bid = blockIdx.x, g = bid & 7, j = bid >> 3, b = j >> 4, s = j & 15;
  const int g4 = l >> 4, bank = (l >> 2) & 3;
  const int gn = (g + 1) & 7;
  const int rq = j >> 3, cq = j & 7;
  const int r8 = j >> 2, hh = r8 >> 1, half = r8 & 1, c = j & 3;
  __amdgpu_buffer_rsrc_t rws;
  unsigned tag, t0 = 0, t1 = 0, t2 = 0;
  unsigned v_h1, s_h1, a_ow, a_or, a_tb, s_fp;
  auto tail_addresses = [&]() __attribute__((always_inline)) {
    v_h1 = (l >> 3) * 4096 + 16 * (l & 7);
    s_h1 = WS_OP + ((gn * 2 + (w >> 2)) * 16 * 4 + hh) * 1024 + (4 * half + (w & 3)) * 128;
    a_ow = L_O + (w >> 2) * 128 + (16 * (w & 3) + 4 * (l & 3) + 2 * (l >> 5) + (g4 & 1)) * 2;
    a_or = L_O + (l & 1) * 128 + 16 * g4;
    a_tb = L_W + (8 * g4 + bank) * 256 + 8 * (l & 3) + 32 * w;
    s_fp = WS_FP + ((c * 64 + gn * 8 + r8) * 128 + 16 * w) * 16;
    asm volatile("" : "+v"(v_h1), "+v"(a_ow), "+v"(a_or), "+v"(a_tb));
    asm volatile("" : "+s"(s_h1), "+s"(s_fp));
  };

  if (__builtin_expect(w < 4, 1)) {
    asm volatile("s_load_dwordx16 %0, %1, 0x0" : "=s"(ka) : "s"(__builtin_amdgcn_kernarg_segment_ptr()));
    SB();
    unsigned wq_v = (l >> 3) * 8192 + 16 * (l & 7), wq_s = (128 * rq + 32 * w) * 8192 + gn * 1024 + 128 * cq;
    unsigned x_v = (l & 4) * 256 + 16 * (l & 3), x_s = 256 * rq + 64 * w;
    unsigned koff = 256 * bank + 64 * g4 + 16 * (l & 3);
    unsigned long long kvoff = ((unsigned long long)(b * 8 + g) * SK + (SK - WIN) + s * 256) * DH * 2;
    asm volatile("" : "+v"(wq_v), "+v"(x_v), "+v"(koff));
    asm volatile("" : "+s"(wq_s), "+s"(x_s), "+s"(kvoff));
    SB();
    asm volatile("s_waitcnt lgkmcnt(0)" : "+s"(ka) : "s"(ka_early));
    const unsigned t_start = (unsigned)__builtin_amdgcn_s_memrealtime();
    rws = __builtin_amdgcn_make_buffer_rsrc((void*)ka[6], 0, WS_END, RSRC_FLAGS);
    const __amdgpu_buffer_rsrc_t rq_ = __builtin_amdgcn_make_buffer_rsrc((void*)ka[3], 0, 512 * 4096 * 2, RSRC_FLAGS);
    const __amdgpu_buffer_rsrc_t rx = __builtin_amdgcn_make_buffer_rsrc((void*)ka[0], 0, 2 * 512 * 2, RSRC_FLAGS);
    v4u wv[4], kk[16], vv[16];
#pragma unroll
    for (int i = 0; i < 4; ++i) { wv[i] = __builtin_amdgcn_raw_buffer_load_b128(rq_, wq_v, wq_s + i * 65536, AUX_NT); SB(); }
    const v4u xv = __builtin_amdgcn_raw_buffer_load_b128(rx, x_v, x_s, 0);
    SB();
    const __amdgpu_buffer_rsrc_t rk = __builtin_amdgcn_make_buffer_rsrc((void*)(ka[1] + kvoff), 0, 65536, RSRC_FLAGS);
    const __amdgpu_buffer_rsrc_t rv = __builtin_amdgcn_make_buffer_rsrc((void*)(ka[2] + kvoff), 0, 65536, RSRC_FLAGS);
    const int rot = s + 5 * g + 3 * b;
    auto stream = [&](int m) __attribute__((always_inline)) {
      const unsigned blk = (((m + rot) & 15) * 4 + w) * 1024;
      if (m < 16) kk[m] = __builtin_amdgcn_raw_buffer_load_b128(rk, koff, blk, AUX_NT);
      else vv[m - 16] = __builtin_amdgcn_raw_buffer_load_b128(rv, 16 * l, blk, AUX_NT);
      SB();
    };
    __builtin_amdgcn_s_barrier();
    SB();
#pragma unroll
    for (int m = 0; m < D0; ++m) stream(m);
    tag = *(const CAS unsigned*)(ka[6] + WS_EPOCH) + 1u;

#pragma unroll
    for (int i = 0; i < 4; ++i) { *(LAS v4u*)(lds + L_W + (4 * w + i) * 1024 + 16 * l) = wv[i]; SB(); stream(D0 + i); }
    *(LAS v4u*)(lds + L_X + (l & 4) * 64 + 64 * w + 16 * (l & 3)) = xv;
    asm volatile("" :: "v"(cpf));
    SB();
    stream(D0 + 4);
    lds_barrier();
    {
      LAS unsigned char* tb = lds + L_W + (8 * g4 + bank) * 128 + 8 * (l & 3) + 32 * w;
      v4u f[4], xa[4];
#pragma unroll
      for (int ks = 0; ks < 4; ++ks) { f[ks] = tr_frag(tb + ks * 32 * 128, 4 * 128); xa[ks] = *(LAS v4u*)(lds + L_X + (l & 1) * 256 + 64 * ks + 16 * g4); }
      SB();
      v4f qa = mfma16(f[0], xa[0], v4f{0.f, 0.f, 0.f, 0.f}), qb = mfma16(f[1], xa[1], v4f{0.f, 0.f, 0.f, 0.f});
      qa = mfma16(f[2], xa[2], qa); qb = mfma16(f[3], xa[3], qb);
      if ((l & 15) < 2) *(LAS v4u*)(lds + L_MRG + (l & 1) * 256 + 64 * w + 16 * g4) = pack4<0x80u>(qa + qb, tag);
    }
    lds_barrier();
    tail_addresses();
    unsigned a_qf = L_Q + (l & 3) * 256 + 16 * (4 * g4 + bank);
    unsigned a_mw = L_MRG + (w * 4 * 128 + 8 * (l & 15) + 2 * g4) * 4, a_mr = L_MRG + (w * 128 + 16 * (l >> 3) + 4 * (l & 3)) * 4, a_ls = L_SUM + 4 * w;
    unsigned s_op = WS_OP + (((g * 2 + b) * 16 + s) * 4 + w) * 1024;
    unsigned long long m_b0 = __ballot(bank == 0), m_ml = __ballot(l & 4);
    asm volatile("" : "+v"(a_qf), "+v"(a_mw), "+v"(a_mr), "+v"(a_ls));
    asm volatile("" : "+s"(s_op), "+s"(m_b0), "+s"(m_ml));

    v4u qg[2];
    auto gather = [&]() __attribute__((always_inline)) {
      MEMBAR();
#pragma unroll
      for (int i = 0; i < 2; ++i)
        qg[i] = __builtin_amdgcn_raw_buffer_load_b128(rws, (l >> 5) * 2048 + 16 * (l & 31), WS_QP + (g * 2 + b) * 8192 + i * 4096 + w * 512, AUX_SC1);
      SB();
    };
    auto gather_bad = [&]() __attribute__((always_inline)) -> bool { return !__all(((badtag(qg[0], tag) | badtag(qg[1], tag)) & 0xffffu) == 0); };
#define STEP(m) if constexpr ((m) >= D0 + 5 && (m) < QPOS) { \
      if constexpr ((m) == GPOS) gather(); \
      WAITVM(DEPTH - 1 + (((m) >= GPOS && (m) - DEPTH < GPOS) ? 2 : 0)); \
      stream(m); }
#define STEP4(m) STEP(m) STEP(m + 1) STEP(m + 2) STEP(m + 3)
    STEP4(4) STEP4(8) STEP4(12) STEP4(16) STEP4(20) STEP4(24) STEP4(28)
#define LATE(k) if constexpr (QPOS + (k) < 32) { SB(); stream(QPOS + (k)); }
    if (gather_bad()) {
#pragma unroll 1
      for (unsigned spin = 0; spin < SPIN_CAP; ++spin) { gather(); if (!gather_bad()) break; }
    }
    {
      const v4f a = unpack4(qg[0]) + unpack4(qg[1]);
      const float e0 = fold32(a[0], a[2]), e1 = fold32(a[1], a[3]);
      *(LAS unsigned*)(lds + L_Q + w * 256 + 8 * (l & 31) + 4 * (l >> 5)) = pkbf(e0, e1);
    }
    LATE(0)
    lds_barrier();

    const v4u qf = *(LAS v4u*)(lds + a_qf);
    float sc[16];
    {
      v4f dq[16];
#pragma unroll
      for (int i = 0; i < 16; ++i) { dq[i] = mfma16(kk[i], qf, v4f{0.f, 0.f, 0.f, 0.f}); if (i == 7) { LATE(1) } }
      LATE(2)
#pragma unroll
      for (int i = 0; i < 16; ++i) sc[i] = dq[i][0] + dpp<SHL4>(dq[i][1]);
      LATE(3)
#pragma unroll
      for (int i = 0; i < 16; ++i) sc[i] += dpp<SHL8>(dq[i][2]);
      LATE(4)
#pragma unroll
      for (int i = 0; i < 16; ++i) sc[i] += dpp<SHL12>(dq[i][3]);
      PIN8(sc); PIN8((sc + 8));
      LATE(5)
    }
    {
      unsigned u[8];
#pragma unroll
      for (int i = 0; i < 8; ++i) u[i] = pkbf(sc[2 * i], sc[2 * i + 1]);
      PIN8(u);
#pragma unroll
      for (int i = 0; i < 8; ++i) { const v2f t = v2f{F(u[i] << 16), F(u[i] & 0xffff0000u)} * 0.08838834764831845f; sc[2 * i] = t[0]; sc[2 * i + 1] = t[1]; }
      SB();
#pragma unroll
      for (int i = 0; i < 8; ++i) u[i] = pkbf(sc[2 * i], sc[2 * i + 1]);
      PIN8(u);
#pragma unroll
      for (int i = 0; i < 8; ++i) { sc[2 * i] = F(u[i] << 16); sc[2 * i + 1] = F(u[i] & 0xffff0000u); }
    }
    float mx = sc[0];
#pragma unroll
    for (int i = 1; i < 16; ++i) mx = vmax(mx, sc[i]);
    LATE(6)
    mx = allmax32(allmax16(mx));
    if (l < 4) *(LAS float*)(lds + L_MAX + (w * 4 + l) * 4) = mx;
    LATE(7)
    lds_barrier();
    constexpr float CS = 1.4426950408889634f;
    const LAS float* pm = (const LAS float*)(lds + L_MAX) + (l & 3);
    const float M2 = vmax(vmax(pm[0], pm[4]), vmax(pm[8], pm[12])) * CS;
#pragma unroll
    for (int i = 0; i < 16; i += 2) {
      const v2f t = v2f{sc[i], sc[i + 1]} * CS - M2;
      sc[i] = __builtin_amdgcn_exp2f(t[0]); sc[i + 1] = __builtin_amdgcn_exp2f(t[1]);
    }
    v4u pf[2][4];
#pragma unroll
    for (int u = 0; u < 2; ++u)
#pragma unroll
      for (int m = 0; m < 4; ++m) pf[u][0][m] = sel(m_b0, pkbf(sc[8 * u + 2 * m], sc[8 * u + 2 * m + 1]), 0u);
    const v4u ones = {0x3f803f80u, 0x3f803f80u, 0x3f803f80u, 0x3f803f80u};
    const v4f lsum = mfma16(pf[1][0], ones, mfma16(pf[0][0], ones, v4f{0.f, 0.f, 0.f, 0.f}));
#pragma unroll
    for (int u = 0; u < 2; ++u)
#pragma unroll
      for (int m = 0; m < 4; ++m) {
        pf[u][1][m] = U(dpp<SHR4>(F(pf[u][0][m])));
        pf[u][2][m] = U(dpp<SHR8>(F(pf[u][0][m])));
        pf[u][3][m] = U(dpp<SHR12>(F(pf[u][0][m])));
      }
    if (l == 0) *(LAS v4f*)(lds + L_SUM + 16 * w) = lsum;
    const unsigned Mw = __builtin_amdgcn_readlane(U(M2), w);

    v4f acc[2] = {v4f{0.f, 0.f, 0.f, 0.f}, v4f{0.f, 0.f, 0.f, 0.f}};
#pragma unroll
    for (int u = 0; u < 2; ++u) {
      v4u vt[8];
#pragma unroll
      for (int m = 0; m < 4; ++m) {
#pragma unroll
        for (int d = 0; d < 8; ++d) vt[d][m] = __builtin_amdgcn_perm(vv[8 * u + 2 * m + 1][d >> 1], vv[8 * u + 2 * m][d >> 1], (d & 1) ? 0x07060302u : 0x05040100u);
        if (m < 3) SB();
      }
#pragma unroll
      for (int d = 0; d < 8; ++d) acc[d & 1] = mfma16(pf[u][d >> 1], vt[d], acc[d & 1]);
      SB();
    }
#pragma unroll
    for (int h = 0; h < 4; ++h) *(LAS v2f*)(lds + a_mw + h * 512) = v2f{acc[0][h], acc[1][h]};
    lds_barrier();
    {
      const LAS float* pl = (const LAS float*)(lds + a_ls);
      const v4f ps = {pl[0], pl[4], pl[8], pl[12]};
      const LAS v4f* src = (const LAS v4f*)(lds + a_mr);
      const v4f o0 = src[0], o1 = src[128], o2 = src[256], o3 = src[384];
      SB();
      const v4u pk = pack4<0u>((o0 + o1) + (o2 + o3), tag);
      __builtin_amdgcn_raw_buffer_store_b128(v4u{sel(m_ml, Mw, pk[0]), pk[1], sel(m_ml, U((ps[0] + ps[1]) + (ps[2] + ps[3])), pk[2]), pk[3]}, rws, 16 * l, s_op, AUX_SC1);
    }
    while ((unsigned)__builtin_amdgcn_s_memrealtime() - t_start < T_FIRST_LOOK) {}
    SB();
  } else {
    SB();
    __builtin_amdgcn_s_barrier();
    SB();
    asm volatile("s_load_dwordx16 %0, %1, 0x0\n\ts_waitcnt lgkmcnt(0)" : "=&s"(ka) : "s"(__builtin_amdgcn_kernarg_segment_ptr()), "s"(ka_early));
    rws = __builtin_amdgcn_make_buffer_rsrc((void*)ka[6], 0, WS_END, RSRC_FLAGS);
    tag = *(const CAS unsigned*)(ka[6] + WS_EPOCH) + 1u;
    tail_addresses();
    __builtin_amdgcn_s_barrier();
    lds_barrier();
    if (w == 4 && l < 32)
      __builtin_amdgcn_raw_buffer_store_b128(*(LAS v4u*)(lds + L_MRG + 16 * l), rws, (l >> 4) * 8192 + 16 * (l & 15), WS_QP + (gn * 8 + rq) * 2048 + 256 * cq, AUX_SC1);
    lds_barrier();
    __builtin_amdgcn_s_barrier();
    const unsigned wo_s = (gn * 512 + 64 * r8 + 16 * (w & 3)) * 1024 + 256 * c;
    t0 = __builtin_amdgcn_raw_buffer_load_b32(rws, (l & 3) * 128, WS_FP + ((c * 64 + gn * 8 + r8) * 128 + 32 * (w - 4)) * 16, 0);
    t1 = __builtin_amdgcn_raw_buffer_load_b32(__builtin_amdgcn_make_buffer_rsrc((void*)ka[5], 0, (w == 4 && j < 8) ? 2048 : 0, RSRC_FLAGS), (l & 1) * 1024, (g * 8 + j) * 16, 0);
    t2 = __builtin_amdgcn_raw_buffer_load_b32(__builtin_amdgcn_make_buffer_rsrc((void*)ka[4], 0, w == 5 ? 4096 * 512 * 2 : 0, RSRC_FLAGS), 0, wo_s, AUX_NT);
    asm volatile("" :: "v"(cpf));
    SB();
    __builtin_amdgcn_s_barrier();
    const __amdgpu_buffer_rsrc_t ro = __builtin_amdgcn_make_buffer_rsrc((void*)ka[4], 0, 4096 * 512 * 2, RSRC_FLAGS);
#pragma unroll
    for (int i = 0; i < 4; ++i)
      __builtin_amdgcn_raw_ptr_buffer_load_lds(ro, lds + L_W + (w & 3) * 4096 + i * 1024, 16, g4 * 1024 + 16 * (l & 15), wo_s + i * 4096, 0, AUX_NT);
    SB();
  }

  __builtin_amdgcn_s_barrier();
  SB();
  v4u wf[2];
  {
    v4u d[2];
    auto poll = [&]() __attribute__((always_inline)) {
      MEMBAR();
#pragma unroll
      for (int i = 0; i < 2; ++i) d[i] = __builtin_amdgcn_raw_buffer_load_b128(rws, v_h1, s_h1 + i * 32768, AUX_SC1);
      SB();
    };
    auto poll_bad = [&]() __attribute__((always_inline)) -> bool { return !__all(((badtag(d[0], tag) | badtag(d[1], tag)) & 0xffffu) == 0); };
    poll();
    asm volatile("s_waitcnt vmcnt(2)" ::: "memory");
    asm volatile("" :: "v"(t0), "v"(t1), "v"(t2));
    lds_barrier();
#pragma unroll
    for (int ks = 0; ks < 2; ++ks) wf[ks] = tr_frag(lds + a_tb + ks * 32 * 256, 4 * 256);
    SB();
    if (poll_bad()) {
#pragma unroll 1
      for (unsigned spin = 0; spin < SPIN_CAP; ++spin) { poll(); if (!poll_bad()) break; }
    }
    const float m0 = dpp<SHL4>(F(d[0][0])), m1 = dpp<SHL4>(F(d[1][0])), l0 = dpp<SHL4>(F(d[0][2])), l1 = dpp<SHL4>(F(d[1][2]));
    float Mx = allmax16(allmax32(vmax(m0, m1)));
    Mx = vmax(Mx, dpp<ROR8>(Mx));
    const float w0 = __builtin_amdgcn_exp2f(m0 - Mx), w1 = __builtin_amdgcn_exp2f(m1 - Mx);
    const v4f a = unpack4(d[0]) * w0 + unpack4(d[1]) * w1;
    float el = l0 * w0 + l1 * w1;
    const float u0 = fold32(a[0], a[2]), u1 = fold32(a[1], a[3]);
    el = fold32(el, el);
    float e = fold16(u0, u1);
    el = fold16(el, el);
    e += dpp<ROR8>(e);
    el += dpp<ROR8>(el);
    e *= __builtin_amdgcn_rcpf(el);
    if (!(l & 12)) *(LAS unsigned short*)(lds + a_ow) = (unsigned short)pkbf(e, e);
  }
  lds_barrier();
  {
    const v4u a0 = *(const LAS v4u*)(lds + a_or), a1 = *(const LAS v4u*)(lds + a_or + 64);
    const v4f f = mfma16(a1, wf[1], mfma16(a0, wf[0], v4f{0.f, 0.f, 0.f, 0.f}));
    if (l < 16) __builtin_amdgcn_raw_buffer_store_b128(v4u{U(f[0]), tag, U(f[1]), tag}, rws, 16 * l, s_fp, AUX_SC1);
  }
  if (j < 8) {
    const int f = g * 8 + j, fc = f >> 4, col0 = 8 * (f & 15);
    const unsigned vo = (l >> 3) * 2048 + 16 * (l & 7);
    const unsigned so = WS_FP + ((fc * 64 + 8 * w) * 128 + col0) * 16;
    v4u d;
    WAITVM(0);
#pragma unroll 1
    for (unsigned spin = 0; spin < SPIN_CAP; ++spin) {
      MEMBAR();
      d = __builtin_amdgcn_raw_buffer_load_b128(rws, vo, so, AUX_SC1);
      if (__all(badtag(d, tag) == 0)) break;
    }
    float r = fold32(F(d[0]), F(d[2]));
    r = fold16(r, r);
    r += dpp<ROR8>(r);
    *(LAS float*)(lds + L_MRG + ((l >> 5) * 8 + (l & 7)) * 32 + 4 * w) = r;
    lds_barrier();
    if (w == 0 && l < 16) {
      const v4f t = *(LAS v4f*)(lds + L_MRG + 32 * l) + *(LAS v4f*)(lds + L_MRG + 32 * l + 16);
      const float z = (t[0] + t[1]) + (t[2] + t[3]);
      ((GAS unsigned short*)ka[5])[(l >> 3) * 512 + 128 * fc + col0 + (l & 7)] = (unsigned short)pkbf(z, z);
      if (f == 0 && l == 0) __builtin_amdgcn_raw_buffer_store_b32(tag, rws, 0, WS_EPOCH, AUX_SC1);
    }
  }
}

extern "C" void kernel_launch(void* const* d_in, const int* in_sizes, int n_in, void* d_out, int out_size, void* d_ws, size_t ws_size,
                              hipStream_t stream) {
  (void)in_sizes; (void)n_in; (void)out_size; (void)ws_size;
  decode_fused<<<256, 512, 0, stream>>>(d_in[0], d_in[1], d_in[2], d_in[3], d_in[4], d_out, d_ws, 0ull);
}
```
